```python
import torch

def _softmax(x, axis=-1):
    return torch.softmax(x, dim=axis)
def _gelu(x):
    return torch.nn.functional.gelu(x, approximate="tanh")
def _silu(x):
    return torch.nn.functional.silu(x)
def _sigmoid(x):
    return torch.sigmoid(x)
def _layernorm(x, axis=-1, eps=1e-5):
    m = x.mean(dim=axis, keepdim=True)
    v = x.var(dim=axis, keepdim=True, unbiased=False)
    return (x - m) / torch.sqrt(v + eps)
def _scatter_add(tgt, idx, src, axis=0):
    return tgt.clone().index_add_(axis, idx, src)
def _segment_sum(src, seg, n):
    out = torch.zeros((n,) + src.shape[1:], dtype=src.dtype)
    return out.index_add_(0, seg, src)
def _pad(x, pw):
    flat = tuple(v for pair in reversed(pw) for v in pair)
    return torch.nn.functional.pad(x, flat)

kernel_name = 'standalone__gqa_block_cached__near_ridge'

def setup_inputs():

    _w = (lambda a: (lambda t: t.to(torch.bfloat16) if t.is_floating_point() else t)(torch.as_tensor(a)))
    B, Sq, D = 8, 144, 512
    Hq, Hkv, Sk, Dh = 32, 32, 1024, 128
    assert Hq % Hkv == 0, (Hq, Hkv)
    return {
        "x": _w((lambda *s: torch.randn(*s, dtype=torch.bfloat16) * 0.8)(B, Sq, D)),
        "k_cache": _w((lambda *s: torch.randn(*s, dtype=torch.bfloat16) * 0.8)(B, Hkv, Sk, Dh)),
        "v_cache": _w((lambda *s: torch.randn(*s, dtype=torch.bfloat16) * 0.05)(B, Hkv, Sk, Dh)),
        "wq": _w((lambda *s: torch.randn(*s, dtype=torch.bfloat16) * 0.8)(D, Hq * Dh) * (D ** -0.5)),
        "wo": _w((lambda *s: torch.randn(*s, dtype=torch.bfloat16) * 0.05)(Hq * Dh, D) * ((Hq * Dh) ** -0.5)),
    }

def reference(x, k_cache, v_cache, wq, wo):
    B, Sq, D = x.shape
    Hq, Dh = 32, 128
    Hkv = k_cache.shape[1]
    rep = Hq // Hkv
    q = (lambda x, *d: x.permute(*d))((x @ wq).reshape(B, Sq, Hq, Dh), 0, 2, 1, 3)
    k = (lambda x, n, axis: torch.repeat_interleave(x, n, dim=axis))(k_cache, rep, axis=1)
    v = (lambda x, n, axis: torch.repeat_interleave(x, n, dim=axis))(v_cache, rep, axis=1)
    scores = torch.einsum("bhqd,bhkd->bhqk", q, k) * (Dh ** -0.5)
    p = _softmax(scores, axis=-1)
    o = torch.einsum("bhqk,bhkd->bhqd", p, v)
    return (lambda x, *d: x.permute(*d))(o, 0, 2, 1, 3).reshape(B, Sq, Hq * Dh) @ wo
```

```cpp
#include <hip/hip_runtime.h>
#include <hip/hip_bf16.h>
#include <stdint.h>
#include <utility>

typedef __bf16 bf16x8 __attribute__((ext_vector_type(8)));
typedef __bf16 bf16x4 __attribute__((ext_vector_type(4)));
typedef __bf16 bf16x2 __attribute__((ext_vector_type(2)));
typedef short s16x4 __attribute__((ext_vector_type(4)));
typedef float f32x4 __attribute__((ext_vector_type(4)));
typedef float f32x2 __attribute__((ext_vector_type(2)));
typedef unsigned u32x4 __attribute__((ext_vector_type(4)));
typedef unsigned u32x2 __attribute__((ext_vector_type(2)));
#define LAS __attribute__((address_space(3)))
#define GAS __attribute__((address_space(1)))
#define DI __device__ __forceinline__

constexpr int NB = 8, SQ = 144, DM = 512, NH = 32, SK = 1024, DH = 128;
constexpr int SLOT = 16384, NSLOT = 9, MISC_OFF = NSLOT * SLOT, LDS_BYTES = MISC_OFF + 16384;
#define NW0 4
#define KV_AUX 2
constexpr int NKV0 = NSLOT - NW0;
constexpr float QSCALE = 0.088388347648318440f * 1.4426950408889634f;
constexpr float THR = 8.0f;

DI f32x4 mfma32(bf16x8 a, bf16x8 b, f32x4 c) { return __builtin_amdgcn_mfma_f32_16x16x32_bf16(a, b, c, 0, 0, 0); }
DI f32x4 mfma16(bf16x4 a, bf16x4 b, f32x4 c) {
  return __builtin_amdgcn_mfma_f32_16x16x16bf16_1k(__builtin_bit_cast(s16x4, a), __builtin_bit_cast(s16x4, b), c, 0, 0, 0);
}
DI unsigned cvtpk(float lo, float hi) { f32x2 v = {lo, hi}; bf16x2 b = __builtin_convertvector(v, bf16x2); return __builtin_bit_cast(unsigned, b); }
DI float max3f(float a, float b, float c) { float r; asm("v_max3_f32 %0, %1, %2, %3" : "=v"(r) : "v"(a), "v"(b), "v"(c)); return r; }
DI float max2f(float a, float b) { float r; asm("v_max_f32_e32 %0, %1, %2" : "=v"(r) : "v"(a), "v"(b)); return r; }
DI float ex2(float x) { return __builtin_amdgcn_exp2f(x); }
DI bf16x8 lds128(unsigned addr) { return __builtin_bit_cast(bf16x8, *(const volatile LAS u32x4*)(uintptr_t)addr); }
DI bf16x4 ldstr(unsigned addr) { return __builtin_amdgcn_ds_read_tr16_b64_v4bf16((LAS bf16x4*)(uintptr_t)addr); }
DI bf16x8 cat(bf16x4 a, bf16x4 b) { return __builtin_shufflevector(a, b, 0, 1, 2, 3, 4, 5, 6, 7); }
#define BAR() asm volatile("s_waitcnt lgkmcnt(0)\n\ts_barrier" ::: "memory")
DI float rowmax4(float x) {
  auto a = __builtin_amdgcn_permlane16_swap(__float_as_uint(x), __float_as_uint(x), false, false);
  x = max2f(__uint_as_float(a[0]), __uint_as_float(a[1]));
  auto b = __builtin_amdgcn_permlane32_swap(__float_as_uint(x), __float_as_uint(x), false, false);
  return max2f(__uint_as_float(b[0]), __uint_as_float(b[1]));
}
DI bf16x8 pack8(f32x4 a, f32x4 b) {
  u32x4 w = {cvtpk(a[0], a[1]), cvtpk(a[2], a[3]), cvtpk(b[0], b[1]), cvtpk(b[2], b[3])};
  return __builtin_bit_cast(bf16x8, w);
}
DI bf16x4 pack4(f32x4 a) { u32x2 w = {cvtpk(a[0], a[1]), cvtpk(a[2], a[3])}; return __builtin_bit_cast(bf16x4, w); }
DI bf16x8 pack8_contig(f32x4 x, f32x4 y) {
  unsigned x0 = cvtpk(x[0], x[1]), x1 = cvtpk(x[2], x[3]), y0 = cvtpk(y[0], y[1]), y1 = cvtpk(y[2], y[3]);
  auto a0 = __builtin_amdgcn_permlane32_swap(x0, y0, false, false);
  auto a1 = __builtin_amdgcn_permlane32_swap(x1, y1, false, false);
  auto b0 = __builtin_amdgcn_permlane16_swap(a0[0], a0[1], false, false);
  auto b1 = __builtin_amdgcn_permlane16_swap(a1[0], a1[1], false, false);
  u32x4 w = {b0[0], b1[0], b0[1], b1[1]};
  return __builtin_bit_cast(bf16x8, w);
}
template <int N> DI void wait_vm() { asm volatile("s_waitcnt vmcnt(%0)" ::"i"(N) : "memory"); }
DI void wait_vm8(int tiles) {
  switch (tiles) {
    case 0: wait_vm<0>(); break;  case 1: wait_vm<8>(); break;  case 2: wait_vm<16>(); break; case 3: wait_vm<24>(); break;
    case 4: wait_vm<32>(); break; case 5: wait_vm<40>(); break; case 6: wait_vm<48>(); break; case 7: wait_vm<56>(); break;
    default: wait_vm<63>(); break;
  }
}
DI void wait_vm16(int tiles) {
  switch (tiles) {
    case 0: wait_vm<0>(); break; case 1: wait_vm<16>(); break; case 2: wait_vm<32>(); break; case 3: wait_vm<48>(); break;
    default: wait_vm<63>(); break;
  }
}
DI __amdgpu_buffer_rsrc_t mk_rsrc(const void* p, unsigned bytes) { return __builtin_amdgcn_make_buffer_rsrc((void*)p, 0, bytes, 0x00020000); }
template <int AUX> DI void dma16(__amdgpu_buffer_rsrc_t r, unsigned ldsaddr, unsigned voff, unsigned soff) {
  __builtin_amdgcn_raw_ptr_buffer_load_lds(r, (LAS void*)(uintptr_t)ldsaddr, 16, voff, soff, 0, AUX);
}

template <int I> struct IntC { static constexpr int value = I; };
template <class F, int... Is> DI void static_for_(F&& f, std::integer_sequence<int, Is...>) { (f(IntC<Is>{}), ...); }
template <int N, class F> DI void static_for(F&& f) { static_for_(f, std::make_integer_sequence<int, N>{}); }
#define SB() __builtin_amdgcn_sched_barrier(0)
#define RAWBAR() asm volatile("s_barrier" ::: "memory")
DI unsigned opaque(unsigned a) { asm volatile("" : "+v"(a)); return a; }
#define PIN(x) asm volatile("" : "+v"(x))

template <class SlotOf, class Sync, class Post>
DI void gemm8(const unsigned (&vb)[4], int w, const bf16x8 (&B0)[16], const bf16x8 (&B1)[16], const bf16x8 (&Bs)[16],
              f32x4 (&a0)[8], f32x4 (&a1)[8], f32x4 (&as)[2], SlotOf slot_of, Sync sync, Post post) {
  constexpr int PD = 4, NI = 128;
  bf16x8 fr[PD + 1], fs[2];
  unsigned ad[4], ads[2];
  const unsigned vbs0 = ((w & 1) ? vb[2] : vb[0]) + (w >> 1) * 1024, vbs1 = ((w & 1) ? vb[3] : vb[1]) + (w >> 1) * 1024;
  static_for<NI + PD>([&](auto rc) {
    constexpr int r = decltype(rc)::value, p = r - PD;
    if constexpr (r < NI) {
      constexpr int i = r >> 4, kk = (r >> 3) & 1, T = r & 7;
      if constexpr ((r & 15) == 0) {
        sync(i);
        unsigned so = slot_of(i);
        asm volatile("" : "+s"(so));
#pragma unroll
        for (int t = 0; t < 4; ++t) ad[t] = vb[t] + so;
        ads[0] = vbs0 + so;
        ads[1] = vbs1 + so;
      }
      const unsigned a = ad[T & 3] + kk * 8192 + (T >> 2) * 1024;
      fr[r % (PD + 1)] = cat(ldstr(a), ldstr(a + 4096));
      if constexpr ((T & 3) == 3) fs[T >> 2] = cat(ldstr(ads[T >> 2] + kk * 8192), ldstr(ads[T >> 2] + kk * 8192 + 4096));
    }
    if constexpr (p >= 0) {
      constexpr int ks = p >> 3, T = p & 7;
      a0[T] = mfma32(fr[p % (PD + 1)], B0[ks], a0[T]);
      a1[T] = mfma32(fr[p % (PD + 1)], B1[ks], a1[T]);
      PIN(a0[T]); PIN(a1[T]);
      if constexpr ((T & 3) == 3) { as[T >> 2] = mfma32(fs[T >> 2], Bs[ks], as[T >> 2]); PIN(as[T >> 2]); }
      if constexpr ((p & 15) == 15) post(IntC<(p >> 4)>{});
    }
    SB();
  });
}

constexpr int wslot(int i) { return NKV0 + ((i + NW0 * 8 - 8) % NW0); }
template <bool V> struct BoolC { static constexpr bool value = V; };

__global__ __launch_bounds__(448, 2) void attn_kernel(const uint16_t* __restrict__ x, const uint16_t* __restrict__ kc,
                                                      const uint16_t* __restrict__ vc, const uint16_t* __restrict__ wq,
                                                      u32x4* __restrict__ ofrag) {
  extern __shared__ __attribute__((aligned(16))) char lds[];
  const int tid = threadIdx.x, lane = tid & 63;
  const int wave = __builtin_amdgcn_readfirstlane(tid >> 6);
  const int bid = blockIdx.x;
  const int h = 4 * (bid & 7) + ((bid >> 3) & 3), b = bid >> 5;
  const unsigned ldsb = (unsigned)(uintptr_t)(LAS char*)lds;

  if (wave == 4) {
    const __amdgpu_buffer_rsrc_t rw = mk_rsrc(wq, DM * NH * DH * 2);
    const int r8 = lane >> 3;
    const unsigned voff = ((r8 & 3) + 8 * (r8 >> 2)) * 8192u + (((lane & 7) ^ (r8 & 6)) << 4);
    auto issue = [&](int i) {
      const unsigned dst = ldsb + wslot(i) * SLOT;
      const unsigned src = (unsigned)i * 64u * 8192u + (unsigned)h * 256u;
#pragma unroll
      for (int m = 0; m < 16; ++m) {
        const int rg = m >> 1, c = m & 1;
        dma16<0>(rw, dst + m * 1024, voff, src + (16 * (rg & 1) + 4 * ((rg >> 1) & 1) + 32 * (rg >> 2)) * 8192u + c * 128);
      }
    };
    for (int i = 0; i < NW0; ++i) issue(i);
    for (int c = 0; c < 8; ++c) {
      int last = c - 3 + NW0; if (last < NW0 - 1) last = NW0 - 1; if (last > 7) last = 7;
      wait_vm16(last - c);
      RAWBAR();
      if (c >= 2 && c - 2 + NW0 <= 7) issue(c - 2 + NW0);
    }
    return;
  }
  if (wave > 4) {
    const size_t hb = (size_t)(b * NH + h) * SK * DH;
    const __amdgpu_buffer_rsrc_t rk = mk_rsrc(kc + hb, SK * DH * 2), rv = mk_rsrc(vc + hb, SK * DH * 2);
    const int r8 = lane >> 3, half = wave - 5;
    {
      const __amdgpu_buffer_rsrc_t rw = mk_rsrc(wq, DM * NH * DH * 2);
      const int u = b * 2 + half;
#pragma unroll
      for (int m = 0; m < 8 - NW0; ++m)
        dma16<0>(rw, ldsb + MISC_OFF + 8192 + half * 4096 + (m & 3) * 1024, (lane >> 4) * 8192u + (lane & 15) * 16u,
                 (unsigned)(NW0 * 64 + (u * (8 - NW0) + m) * 4) * 8192u + (unsigned)h * 256u);
    }
    const unsigned voff = r8 * 256u + (((lane & 7) ^ (r8 & 6)) << 4);
    auto issue = [&](int j) {
      const unsigned dst = ldsb + (j % NSLOT) * SLOT + half * 8192;
      const unsigned src = (unsigned)(j >> 1) * 16384u + half * 8192u;
      if (j & 1) {
#pragma unroll
        for (int m = 0; m < 8; ++m) dma16<KV_AUX>(rv, dst + m * 1024, voff, src + (m >> 1) * 2048 + (m & 1) * 128);
      } else {
#pragma unroll
        for (int m = 0; m < 8; ++m) dma16<KV_AUX>(rk, dst + m * 1024, voff, src + (m >> 1) * 2048 + (m & 1) * 128);
      }
    };
    for (int j = 0; j < NKV0; ++j) issue(j);
    for (int c = 0; c < 40; ++c) {
      if (c >= 8) {
        int last = c - 2; if (last < NKV0 - 1) last = NKV0 - 1; if (last > 31) last = 31;
        wait_vm8(last - (c - 8));
      }
      RAWBAR();
      if (c - 1 >= NKV0 && c - 1 < 32) issue(c - 1);
    }
    return;
  }

  const int w = wave, g = lane >> 4, i16 = lane & 15;
  unsigned vb[4];
  {
    const int q = i16 >> 2, p = i16 & 3, r8 = 4 * (g & 1) + q;
#pragma unroll
    for (int t = 0; t < 4; ++t) vb[t] = ldsb + (g >> 1) * 2048 + r8 * 128 + ((t ^ (r8 >> 1)) << 5) + 8 * p;
  }
  bf16x8 Q0[4], Q1[4], Qs[4];
  {
    const GAS bf16x8* xr0 = (const GAS bf16x8*)(x + ((size_t)(b * SQ + 32 * w + i16) * DM + 8 * g));
    const GAS bf16x8* xr1 = xr0 + 16 * DM / 8;
    const GAS bf16x8* xrs = (const GAS bf16x8*)(x + ((size_t)(b * SQ + 128 + i16) * DM + 8 * g));
    bf16x8 xf0[16], xf1[16], xfs[16];
#pragma unroll
    for (int ks = 0; ks < 10; ++ks) { xf0[ks] = xr0[ks * 4]; xf1[ks] = xr1[ks * 4]; xfs[ks] = xrs[ks * 4]; }
    f32x4 a0[8], a1[8], as[2];
#pragma unroll
    for (int T = 0; T < 8; ++T) { a0[T] = f32x4{0, 0, 0, 0}; a1[T] = f32x4{0, 0, 0, 0}; }
    as[0] = f32x4{0, 0, 0, 0}; as[1] = f32x4{0, 0, 0, 0};
    SB();
    gemm8(vb, w, xf0, xf1, xfs, a0, a1, as, [](int i) { return (unsigned)(wslot(i) * SLOT); }, [](int) { RAWBAR(); },
          [&](auto ic) {
            constexpr int i = decltype(ic)::value;
            if constexpr (i < 3) {
#pragma unroll
              for (int ks = 2 * i + 10; ks < 2 * i + 12; ++ks) { xf0[ks] = xr0[ks * 4]; xf1[ks] = xr1[ks * 4]; xfs[ks] = xrs[ks * 4]; }
            }
          });
#pragma unroll
    for (int s = 0; s < 4; ++s) {
      Q0[s] = pack8_contig(a0[2 * s] * QSCALE, a0[2 * s + 1] * QSCALE);
      Q1[s] = pack8_contig(a1[2 * s] * QSCALE, a1[2 * s + 1] * QSCALE);
    }
    *(LAS bf16x8*)(uintptr_t)(ldsb + MISC_OFF + w * 1024 + lane * 16) = pack8_contig(as[0] * QSCALE, as[1] * QSCALE);
  }
  unsigned kb[2];
  {
    const int r8 = i16 & 7;
#pragma unroll
    for (int s = 0; s < 2; ++s) kb[s] = ldsb + (i16 >> 3) * 2048 + r8 * 128 + (((4 * s + g) ^ (r8 & 6)) << 4);
  }
  const unsigned w4k = w * 4096;
  const u32x4 ones_u = {0x3F803F80u, 0x3F803F80u, 0x3F803F80u, 0x3F803F80u};
  const bf16x8 ones8 = __builtin_bit_cast(bf16x8, ones_u);
  const u32x2 ones_u2 = {0x3F803F80u, 0x3F803F80u};
  const bf16x4 ones4 = __builtin_bit_cast(bf16x4, ones_u2);

  f32x4 O0[8], O1[8], Os[8], l0 = {0, 0, 0, 0}, l1 = {0, 0, 0, 0}, ls = {0, 0, 0, 0};
#pragma unroll
  for (int T = 0; T < 8; ++T) { O0[T] = f32x4{0, 0, 0, 0}; O1[T] = f32x4{0, 0, 0, 0}; Os[T] = f32x4{0, 0, 0, 0}; }
  f32x4 nm0 = {0, 0, 0, 0}, nm1 = {0, 0, 0, 0}, nms = {0, 0, 0, 0};
  f32x4 Sa0[2], Sa1[2], Sas, Sb0[2], Sb1[2];

  auto max8 = [&](const f32x4(&S)[2]) {
    return max2f(max3f(max3f(S[0][0], S[0][1], S[0][2]), S[0][3], S[1][0]), max3f(S[1][1], S[1][2], S[1][3]));
  };
  auto max4 = [&](const f32x4& S) { return max2f(max3f(S[0], S[1], S[2]), S[3]); };
  auto fix8 = [&](f32x4(&S)[2], f32x4& nm, f32x4(&O)[8], f32x4& l, bool first) {
    float d = rowmax4(max8(S));
    if (!first) d = fmaxf(d, 0.f);
    S[0] -= d; S[1] -= d; nm -= d;
    if (!first) {
      const float al = ex2(-d);
#pragma unroll
      for (int T = 0; T < 8; ++T) O[T] *= al;
      l *= al;
    }
  };
  auto fix4 = [&](bool first) {
    float d = rowmax4(max4(Sas));
    if (!first) d = fmaxf(d, 0.f);
    Sas -= d; nms -= d;
    if (!first) {
      const float al = ex2(-d);
#pragma unroll
      for (int T = 0; T < 8; ++T) Os[T] *= al;
      ls *= al;
    }
  };

  constexpr int PD = 4;
  bf16x8 fr[8];
  BAR();
#pragma unroll
  for (int s = 0; s < 4; ++s) Qs[s] = lds128(ldsb + MISC_OFF + s * 1024 + lane * 16);
#pragma unroll
  for (int it = 0; it < 2; ++it)
#pragma unroll
    for (int s = 0; s < 4; ++s) {
      const bf16x8 kf = lds128(kb[s & 1] + it * 4096 + (s >> 1) * 1024);
      Sa0[it] = mfma32(kf, Q0[s], s ? Sa0[it] : nm0);
      Sa1[it] = mfma32(kf, Q1[s], s ? Sa1[it] : nm1);
    }
#pragma unroll
  for (int s = 0; s < 4; ++s) Sas = mfma32(lds128(kb[s & 1] + w4k + (s >> 1) * 1024), Qs[s], s ? Sas : nms);
#pragma unroll
  for (int r = 0; r < PD; ++r) fr[r] = lds128(kb[r & 1] + (2 + (r >> 2)) * 4096 + ((r & 3) >> 1) * 1024);
  fix8(Sa0, nm0, O0, l0, true); fix8(Sa1, nm1, O1, l1, true); fix4(true);
  SB();

  int sk = 0, sv = 1;
  auto body = [&](auto lastc) {
    constexpr bool LAST = decltype(lastc)::value;
    unsigned aK[2], aKs[2], aV[4], aVs[4];
    aK[0] = opaque(kb[0] + sk * SLOT); aK[1] = opaque(kb[1] + sk * SLOT);
    sk += 2; if (sk >= NSLOT) sk -= NSLOT;
    unsigned Pa0[4], Pa1[4], Pb0[4], Pb1[4], Pas[2];
    bf16x8 ksf[4]; bf16x4 vsf[8];
    float mx = 0.f, my = 0.f;
    static_for<32>([&](auto pc) {
      constexpr int p = decltype(pc)::value;
      constexpr int r = p + PD, rj = r & 7;
      if constexpr (r < 8) {
        fr[rj] = lds128(aK[rj & 1] + (2 + (rj >> 2)) * 4096 + ((rj & 3) >> 1) * 1024);
      } else if constexpr (r < 16) {
        if constexpr (r == 8) {
          RAWBAR();
#pragma unroll
          for (int t = 0; t < 4; ++t) { aV[t] = opaque(vb[t] + sv * SLOT); aVs[t] = opaque(aV[t] + w4k); }
        }
        const unsigned a = aV[rj & 3] + (rj >> 2) * 1024;
        fr[rj] = cat(ldstr(a), ldstr(a + 4096));
        if constexpr (rj >= 4) {
          constexpr int T = 2 * (rj - 4);
          vsf[T] = ldstr(aVs[T & 3] + (T >> 2) * 1024); vsf[T + 1] = ldstr(aVs[(T + 1) & 3] + (T >> 2) * 1024);
        }
      } else if constexpr (r < 24) {
        if constexpr (!LAST) {
          if constexpr (r == 16) {
            RAWBAR();
            aK[0] = opaque(kb[0] + sk * SLOT); aK[1] = opaque(kb[1] + sk * SLOT);
            aKs[0] = opaque(aK[0] + w4k); aKs[1] = opaque(aK[1] + w4k);
          }
          fr[rj] = lds128(aK[rj & 1] + (rj >> 2) * 4096 + ((rj & 3) >> 1) * 1024);
          if constexpr (rj & 1) ksf[rj >> 1] = lds128(aKs[(rj >> 1) & 1] + (rj >> 2) * 1024);
        }
      } else if constexpr (r < 32) {
        const unsigned a = aV[rj & 3] + 8192 + (rj >> 2) * 1024;
        fr[rj] = cat(ldstr(a), ldstr(a + 4096));
      } else {
        if constexpr (!LAST) fr[rj] = lds128(aK[rj & 1] + (2 + (rj >> 2)) * 4096 + ((rj & 3) >> 1) * 1024);
      }
      constexpr int j = p & 7;
      if constexpr (p < 8) {
        constexpr int it = j >> 2, s = j & 3;
        Sb0[it] = mfma32(fr[j], Q0[s], s ? Sb0[it] : nm0);
        Sb1[it] = mfma32(fr[j], Q1[s], s ? Sb1[it] : nm1);
        PIN(Sb0[it]); PIN(Sb1[it]);
        if constexpr (j < 4) { Pa0[j] = cvtpk(ex2(Sa0[j >> 1][2 * (j & 1)]), ex2(Sa0[j >> 1][2 * (j & 1) + 1])); PIN(Pa0[j]); }
        else { Pa1[j - 4] = cvtpk(ex2(Sa1[(j - 4) >> 1][2 * (j & 1)]), ex2(Sa1[(j - 4) >> 1][2 * (j & 1) + 1])); PIN(Pa1[j - 4]); }
      } else if constexpr (p < 16) {
        const u32x4 p0 = {Pa0[0], Pa0[1], Pa0[2], Pa0[3]}, p1 = {Pa1[0], Pa1[1], Pa1[2], Pa1[3]};
        const bf16x8 P0 = __builtin_bit_cast(bf16x8, p0), P1 = __builtin_bit_cast(bf16x8, p1);
        if constexpr (j == 0) { l0 = mfma32(ones8, P0, l0); l1 = mfma32(ones8, P1, l1); PIN(l0); PIN(l1); }
        O0[j] = mfma32(fr[j], P0, O0[j]);
        O1[j] = mfma32(fr[j], P1, O1[j]);
        PIN(O0[j]); PIN(O1[j]);
        if constexpr (j < 2) { Pas[j] = cvtpk(ex2(Sas[2 * j]), ex2(Sas[2 * j + 1])); PIN(Pas[j]); }
        if constexpr (j == 2) { mx = max3f(Sb0[0][0], Sb0[0][1], Sb0[0][2]); my = max3f(Sb1[0][0], Sb1[0][1], Sb1[0][2]); PIN(mx); PIN(my); }
        if constexpr (j == 3) { mx = max3f(mx, Sb0[0][3], Sb0[1][0]); my = max3f(my, Sb1[0][3], Sb1[1][0]); PIN(mx); PIN(my); }
        if constexpr (j >= 4) {
          const u32x2 ps = {Pas[0], Pas[1]};
          const bf16x4 Ps = __builtin_bit_cast(bf16x4, ps);
          constexpr int T = 2 * (j - 4);
          Os[T] = mfma16(vsf[T], Ps, Os[T]); Os[T + 1] = mfma16(vsf[T + 1], Ps, Os[T + 1]);
          PIN(Os[T]); PIN(Os[T + 1]);
          if constexpr (j == 7) { ls = mfma16(ones4, Ps, ls); PIN(ls); }
        }
        if constexpr (j == 4) { mx = max3f(mx, Sb0[1][1], Sb0[1][2]); my = max3f(my, Sb1[1][1], Sb1[1][2]); PIN(mx); PIN(my); }
        if constexpr (j == 5) { mx = max3f(mx, Sb0[1][3], Sb1[1][3]); mx = max2f(mx, my); PIN(mx); }
        if constexpr (j == 7) {
          SB();
          if (__builtin_expect(__any(mx > THR), 0)) { fix8(Sb0, nm0, O0, l0, false); fix8(Sb1, nm1, O1, l1, false); }
        }
      } else if constexpr (p < 24) {
        if constexpr (!LAST) {
          constexpr int it = j >> 2, s = j & 3;
          Sa0[it] = mfma32(fr[j], Q0[s], s ? Sa0[it] : nm0);
          Sa1[it] = mfma32(fr[j], Q1[s], s ? Sa1[it] : nm1);
          PIN(Sa0[it]); PIN(Sa1[it]);
          if constexpr (j & 1) { Sas = mfma32(ksf[j >> 1], Qs[j >> 1], (j >> 1) ? Sas : nms); PIN(Sas); }
        }
        if constexpr (j < 4) { Pb0[j] = cvtpk(ex2(Sb0[j >> 1][2 * (j & 1)]), ex2(Sb0[j >> 1][2 * (j & 1) + 1])); PIN(Pb0[j]); }
        else { Pb1[j - 4] = cvtpk(ex2(Sb1[(j - 4) >> 1][2 * (j & 1)]), ex2(Sb1[(j - 4) >> 1][2 * (j & 1) + 1])); PIN(Pb1[j - 4]); }
      } else {
        const u32x4 p0 = {Pb0[0], Pb0[1], Pb0[2], Pb0[3]}, p1 = {Pb1[0], Pb1[1], Pb1[2], Pb1[3]};
        const bf16x8 P0 = __builtin_bit_cast(bf16x8, p0), P1 = __builtin_bit_cast(bf16x8, p1);
        if constexpr (j == 0) { l0 = mfma32(ones8, P0, l0); l1 = mfma32(ones8, P1, l1); PIN(l0); PIN(l1); }
        O0[j] = mfma32(fr[j], P0, O0[j]);
        O1[j] = mfma32(fr[j], P1, O1[j]);
        PIN(O0[j]); PIN(O1[j]);
        if constexpr (!LAST) {
          if constexpr (j == 2) { mx = max3f(Sa0[0][0], Sa0[0][1], Sa0[0][2]); my = max3f(Sa1[0][0], Sa1[0][1], Sa1[0][2]); PIN(mx); PIN(my); }
          if constexpr (j == 3) { mx = max3f(mx, Sa0[0][3], Sa0[1][0]); my = max3f(my, Sa1[0][3], Sa1[1][0]); PIN(mx); PIN(my); }
          if constexpr (j == 4) { mx = max3f(mx, Sa0[1][1], Sa0[1][2]); my = max3f(my, Sa1[1][1], Sa1[1][2]); PIN(mx); PIN(my); }
          if constexpr (j == 5) { mx = max3f(mx, Sa0[1][3], Sa1[1][3]); my = max3f(my, Sas[0], Sas[1]); PIN(mx); PIN(my); }
          if constexpr (j == 6) { mx = max3f(mx, Sas[2], Sas[3]); mx = max2f(mx, my); PIN(mx); }
          if constexpr (j == 7) {
            SB();
            if (__builtin_expect(__any(mx > THR), 0)) {
              fix8(Sa0, nm0, O0, l0, false); fix8(Sa1, nm1, O1, l1, false); fix4(false);
            }
          }
        }
      }
      SB();
    });
    sv += 2; if (sv >= NSLOT) sv -= NSLOT;
  };
  for (int n = 0; n < 15; ++n) body(BoolC<false>{});
  body(BoolC<true>{});

  u32x4* of = ofrag + (size_t)(b * NH + h) * 36 * 64;
  {
    const float r0 = __builtin_amdgcn_rcpf(l0[0]), r1 = __builtin_amdgcn_rcpf(l1[0]);
#pragma unroll
    for (int s = 0; s < 4; ++s) {
      of[((2 * w) * 4 + s) * 64 + lane] = __builtin_bit_cast(u32x4, pack8(O0[2 * s] * r0, O0[2 * s + 1] * r0));
      of[((2 * w + 1) * 4 + s) * 64 + lane] = __builtin_bit_cast(u32x4, pack8(O1[2 * s] * r1, O1[2 * s + 1] * r1));
    }
  }
  {
    const unsigned cb = ldsb + 5 * SLOT;
#pragma unroll
    for (int T = 0; T < 8; ++T) *(LAS f32x4*)(uintptr_t)(cb + (w * 8 + T) * 1024 + lane * 16) = Os[T];
    *(LAS f32x2*)(uintptr_t)(ldsb + MISC_OFF + 4096 + w * 512 + lane * 8) = f32x2{-nms[0], ls[0]};
    BAR();
    f32x2 ml[4];
#pragma unroll
    for (int u = 0; u < 4; ++u) ml[u] = *(const LAS f32x2*)(uintptr_t)(ldsb + MISC_OFF + 4096 + u * 512 + lane * 8);
    const float M = max2f(max2f(ml[0][0], ml[1][0]), max2f(ml[2][0], ml[3][0]));
    float f[4], L = 0.f;
#pragma unroll
    for (int u = 0; u < 4; ++u) { f[u] = ex2(ml[u][0] - M); L += f[u] * ml[u][1]; }
    const float rL = __builtin_amdgcn_rcpf(L);
    f32x4 o0 = {0, 0, 0, 0}, o1 = {0, 0, 0, 0};
#pragma unroll
    for (int u = 0; u < 4; ++u) {
      o0 += *(const LAS f32x4*)(uintptr_t)(cb + (u * 8 + 2 * w) * 1024 + lane * 16) * (f[u] * rL);
      o1 += *(const LAS f32x4*)(uintptr_t)(cb + (u * 8 + 2 * w + 1) * 1024 + lane * 16) * (f[u] * rL);
    }
    of[(8 * 4 + w) * 64 + lane] = __builtin_bit_cast(u32x4, pack8(o0, o1));
  }
}

__global__ __launch_bounds__(256, 1) void oproj_kernel(const u32x4* __restrict__ ofrag, const uint16_t* __restrict__ wo,
                                                       f32x4* __restrict__ part) {
  extern __shared__ __attribute__((aligned(16))) char lds[];
  const int tid = threadIdx.x, lane = tid & 63;
  const int w = __builtin_amdgcn_readfirstlane(tid >> 6);
  const int bid = blockIdx.x;
  const int hg = bid & 7, nt = (bid >> 3) & 3, b = bid >> 5;
  const unsigned ldsb = (unsigned)(uintptr_t)(LAS char*)lds;
  const int g = lane >> 4, i16 = lane & 15;
  {
    const __amdgpu_buffer_rsrc_t rw = mk_rsrc(wo, NH * DH * DM * 2);
    const int r8 = lane >> 3;
    const unsigned voff = r8 * 1024u + (((lane & 7) ^ (r8 & 6)) << 4);
#pragma unroll
    for (int i = 0; i < 8; ++i)
#pragma unroll
      for (int mm = 0; mm < 4; ++mm) {
        const int m = w * 4 + mm;
        dma16<0>(rw, ldsb + i * SLOT + m * 1024, voff, (unsigned)(hg * 512 + i * 64 + (m >> 1) * 8) * 1024u + nt * 256 + (m & 1) * 128);
      }
  }
  unsigned vb[4];
  {
    const int q = i16 >> 2, p = i16 & 3, r8 = 4 * (g & 1) + q;
#pragma unroll
    for (int t = 0; t < 4; ++t) vb[t] = ldsb + (g >> 1) * 2048 + r8 * 128 + ((t ^ (r8 >> 1)) << 5) + 8 * p;
  }
  bf16x8 f0[16], f1[16], fs[16];
#pragma unroll
  for (int hh = 0; hh < 4; ++hh) {
    const u32x4* of = ofrag + (size_t)(b * NH + hg * 4 + hh) * 36 * 64 + lane;
#pragma unroll
    for (int s = 0; s < 4; ++s) {
      f0[hh * 4 + s] = __builtin_bit_cast(bf16x8, of[((2 * w) * 4 + s) * 64]);
      f1[hh * 4 + s] = __builtin_bit_cast(bf16x8, of[((2 * w + 1) * 4 + s) * 64]);
      fs[hh * 4 + s] = __builtin_bit_cast(bf16x8, of[(8 * 4 + s) * 64]);
    }
  }
  f32x4 a0[8], a1[8], as[2];
#pragma unroll
  for (int T = 0; T < 8; ++T) { a0[T] = f32x4{0, 0, 0, 0}; a1[T] = f32x4{0, 0, 0, 0}; }
  as[0] = f32x4{0, 0, 0, 0}; as[1] = f32x4{0, 0, 0, 0};
  wait_vm<0>();
  BAR();
  gemm8(vb, w, f0, f1, fs, a0, a1, as, [](int i) { return (unsigned)(i * SLOT); }, [](int) {}, [](auto) {});
  f32x4* pp = part + (size_t)((b * 4 + nt) * 8 + hg) * 72 * 64 + lane;
#pragma unroll
  for (int T = 0; T < 8; ++T) { pp[((2 * w) * 8 + T) * 64] = a0[T]; pp[((2 * w + 1) * 8 + T) * 64] = a1[T]; }
  pp[(8 * 8 + 2 * w) * 64] = as[0]; pp[(8 * 8 + 2 * w + 1) * 64] = as[1];
}

__global__ __launch_bounds__(256) void reduce_kernel(const f32x4* __restrict__ part, uint16_t* __restrict__ out) {
  const int lane = threadIdx.x & 63, wid = blockIdx.x * 4 + (threadIdx.x >> 6);
  const int s = wid & 3, rt = (wid >> 2) % 9, bn = (wid >> 2) / 9;
  const f32x4* pp = part + (size_t)bn * 8 * 72 * 64 + (rt * 8 + 2 * s) * 64 + lane;
  f32x4 x = {0, 0, 0, 0}, y = {0, 0, 0, 0};
#pragma unroll
  for (int hg = 0; hg < 8; ++hg) { x += pp[(size_t)hg * 72 * 64]; y += pp[(size_t)hg * 72 * 64 + 64]; }
  const bf16x8 o = pack8_contig(x, y);
  const int b = bn >> 2, nt = bn & 3, g = lane >> 4, i16 = lane & 15;
  *(u32x4*)(out + ((size_t)(b * SQ + rt * 16 + i16) * DM + nt * 128 + s * 32 + g * 8)) = __builtin_bit_cast(u32x4, o);
}

extern "C" void kernel_launch(void* const* d_in, const int* in_sizes, int n_in, void* d_out, int out_size, void* d_ws,
                              size_t ws_size, hipStream_t stream) {
  const uint16_t* x = (const uint16_t*)d_in[0];
  const uint16_t* kc = (const uint16_t*)d_in[1];
  const uint16_t* vc = (const uint16_t*)d_in[2];
  const uint16_t* wq = (const uint16_t*)d_in[3];
  const uint16_t* wo = (const uint16_t*)d_in[4];
  u32x4* ofrag = (u32x4*)d_ws;
  f32x4* part = (f32x4*)((char*)d_ws + (size_t)256 * 36 * 1024);
  (void)hipFuncSetAttribute((const void*)attn_kernel, hipFuncAttributeMaxDynamicSharedMemorySize, LDS_BYTES);
  (void)hipFuncSetAttribute((const void*)oproj_kernel, hipFuncAttributeMaxDynamicSharedMemorySize, 8 * SLOT);
  hipLaunchKernelGGL(attn_kernel, dim3(256), dim3(448), LDS_BYTES, stream, x, kc, vc, wq, ofrag);
  hipLaunchKernelGGL(oproj_kernel, dim3(256), dim3(256), 8 * SLOT, stream, (const u32x4*)ofrag, wo, part);
  hipLaunchKernelGGL(reduce_kernel, dim3(32 * 36 / 4), dim3(256), 0, stream, (const f32x4*)part, (uint16_t*)d_out);
}
```

```cpp
#include <hip/hip_runtime.h>
#include <hip/hip_bf16.h>
#include <stdint.h>
#include <utility>

typedef __bf16 bf16x8 __attribute__((ext_vector_type(8)));
typedef __bf16 bf16x4 __attribute__((ext_vector_type(4)));
typedef __bf16 bf16x2 __attribute__((ext_vector_type(2)));
typedef short s16x4 __attribute__((ext_vector_type(4)));
typedef float f32x16 __attribute__((ext_vector_type(16)));
typedef float f32x4 __attribute__((ext_vector_type(4)));
typedef float f32x2 __attribute__((ext_vector_type(2)));
typedef unsigned u32x4 __attribute__((ext_vector_type(4)));
typedef unsigned u32x2 __attribute__((ext_vector_type(2)));
#define LAS __attribute__((address_space(3)))
#define GAS __attribute__((address_space(1)))
#define DI __device__ __forceinline__

constexpr int NB = 8, SQ = 144, DM = 512, NH = 32, SK = 1024, DH = 128;
constexpr int SLOT = 16384, NSLOT = 9, LDS_BYTES = 163840;
constexpr int NKV0 = 3, STG0 = NKV0 * SLOT, STG = 34 * 1024, MISC_OFF = STG0 + 3 * STG, DUMP_OFF = MISC_OFF + 6144;
DI int stg_off(int i) { return STG0 + ((i + 1) % 3) * STG; }
#define KV_AUX 2
#define WX_AUX 0
#define KV_CAP 24
constexpr size_t WS_FLAG_BYTES = 65536, WS_OF = WS_FLAG_BYTES, WS_PART = WS_OF + (size_t)256 * 36 * 1024, WS_END = WS_PART + (size_t)256 * 72 * 1024;
constexpr float QSCALE = 0.088388347648318440f * 1.4426950408889634f;
constexpr float THR = 8.0f;

DI f32x16 mfmaL(bf16x8 a, bf16x8 b, f32x16 c) { return __builtin_amdgcn_mfma_f32_32x32x16_bf16(a, b, c, 0, 0, 0); }
DI f32x4 mfma32(bf16x8 a, bf16x8 b, f32x4 c) { return __builtin_amdgcn_mfma_f32_16x16x32_bf16(a, b, c, 0, 0, 0); }
DI f32x4 mfma16(bf16x4 a, bf16x4 b, f32x4 c) {
  return __builtin_amdgcn_mfma_f32_16x16x16bf16_1k(__builtin_bit_cast(s16x4, a), __builtin_bit_cast(s16x4, b), c, 0, 0, 0);
}
DI unsigned cvtpk(float lo, float hi) { f32x2 v = {lo, hi}; bf16x2 b = __builtin_convertvector(v, bf16x2); return __builtin_bit_cast(unsigned, b); }
DI float max3f(float a, float b, float c) { return fmaxf(fmaxf(a, b), c); }
DI float max2f(float a, float b) { return fmaxf(a, b); }
DI int imax3(float a, float b, float c) { return max(max(__float_as_int(a), __float_as_int(b)), __float_as_int(c)); }
DI int imax3(int a, float b, float c) { return max(max(a, __float_as_int(b)), __float_as_int(c)); }
DI float ex2(float x) { return __builtin_amdgcn_exp2f(x); }
DI bf16x8 lds128(unsigned addr) { return __builtin_bit_cast(bf16x8, *(const volatile LAS u32x4*)(uintptr_t)addr); }
DI bf16x4 ldstr(unsigned addr) { return __builtin_amdgcn_ds_read_tr16_b64_v4bf16((LAS bf16x4*)(uintptr_t)addr); }
DI bf16x8 cat(bf16x4 a, bf16x4 b) { return __builtin_shufflevector(a, b, 0, 1, 2, 3, 4, 5, 6, 7); }
#define BAR() asm volatile("s_waitcnt lgkmcnt(0)\n\ts_barrier" ::: "memory")
#define RAWBAR() asm volatile("s_barrier" ::: "memory")
#define SB() __builtin_amdgcn_sched_barrier(0)
#define PIN(x) asm volatile("" : "+v"(x))
template <int I> struct IntC { static constexpr int value = I; };
template <bool V> struct BoolC { static constexpr bool value = V; };
template <class F, int... Is> DI void static_for_(F&& f, std::integer_sequence<int, Is...>) { (f(IntC<Is>{}), ...); }
template <int N, class F> DI void static_for(F&& f) { static_for_(f, std::make_integer_sequence<int, N>{}); }
DI float xmax32(float x) {
  auto b = __builtin_amdgcn_permlane32_swap(__float_as_uint(x), __float_as_uint(x), false, false);
  return max2f(__uint_as_float(b[0]), __uint_as_float(b[1]));
}
DI float xmax16(float x) {
  auto a = __builtin_amdgcn_permlane16_swap(__float_as_uint(x), __float_as_uint(x), false, false);
  return max2f(__uint_as_float(a[0]), __uint_as_float(a[1]));
}
DI float xadd32(float x) {
  auto b = __builtin_amdgcn_permlane32_swap(__float_as_uint(x), __float_as_uint(x), false, false);
  return __uint_as_float(b[0]) + __uint_as_float(b[1]);
}
DI float xadd16(float x) {
  auto b = __builtin_amdgcn_permlane16_swap(__float_as_uint(x), __float_as_uint(x), false, false);
  return __uint_as_float(b[0]) + __uint_as_float(b[1]);
}
DI bf16x8 pack8(f32x4 a, f32x4 b) {
  u32x4 w = {cvtpk(a[0], a[1]), cvtpk(a[2], a[3]), cvtpk(b[0], b[1]), cvtpk(b[2], b[3])};
  return __builtin_bit_cast(bf16x8, w);
}
template <int Q> DI f32x4 quad(const f32x16& v) { return f32x4{v[4 * Q], v[4 * Q + 1], v[4 * Q + 2], v[4 * Q + 3]}; }
DI bf16x8 pack8_contig16(f32x4 x, f32x4 y) {
  unsigned x0 = cvtpk(x[0], x[1]), x1 = cvtpk(x[2], x[3]), y0 = cvtpk(y[0], y[1]), y1 = cvtpk(y[2], y[3]);
  auto a0 = __builtin_amdgcn_permlane32_swap(x0, y0, false, false);
  auto a1 = __builtin_amdgcn_permlane32_swap(x1, y1, false, false);
  auto b0 = __builtin_amdgcn_permlane16_swap(a0[0], a0[1], false, false);
  auto b1 = __builtin_amdgcn_permlane16_swap(a1[0], a1[1], false, false);
  u32x4 w = {b0[0], b1[0], b0[1], b1[1]};
  return __builtin_bit_cast(bf16x8, w);
}
DI bf16x8 pack8_contig32(f32x4 x, f32x4 y) {
  unsigned x0 = cvtpk(x[0], x[1]), x1 = cvtpk(x[2], x[3]), y0 = cvtpk(y[0], y[1]), y1 = cvtpk(y[2], y[3]);
  auto a0 = __builtin_amdgcn_permlane32_swap(x0, y0, false, false);
  auto a1 = __builtin_amdgcn_permlane32_swap(x1, y1, false, false);
  u32x4 w = {a0[0], a1[0], a0[1], a1[1]};
  return __builtin_bit_cast(bf16x8, w);
}
template <int N> DI void wait_vm() { asm volatile("s_waitcnt vmcnt(%0)" ::"i"(N) : "memory"); }
DI void wait_vm8(int tiles) {
  switch (tiles) {
    case 0: wait_vm<0>(); break;  case 1: wait_vm<8>(); break;  case 2: wait_vm<16>(); break; case 3: wait_vm<24>(); break;
    case 4: wait_vm<32>(); break; case 5: wait_vm<40>(); break; case 6: wait_vm<48>(); break; case 7: wait_vm<56>(); break;
    default: wait_vm<63>(); break;
  }
}
DI int vm_outstanding() {
  const unsigned v = __builtin_amdgcn_s_getreg(7 | (31 << 11));
  return (v & 15) | ((v >> 22) & 3) << 4;
}
DI void wait_flags(GAS unsigned* fp) {
  for (int it = 0; it < (1 << 20); ++it) {
    if (__all(__hip_atomic_load(fp, __ATOMIC_RELAXED, __HIP_MEMORY_SCOPE_AGENT) == 1u)) break;
    __builtin_amdgcn_s_sleep(1);
  }
}
DI __amdgpu_buffer_rsrc_t mk_rsrc(const void* p, unsigned bytes) { return __builtin_amdgcn_make_buffer_rsrc((void*)p, 0, bytes, 0x00020000); }
template <int AUX> DI void dma16(__amdgpu_buffer_rsrc_t r, unsigned ldsaddr, unsigned voff, unsigned soff) {
  __builtin_amdgcn_raw_ptr_buffer_load_lds(r, (LAS void*)(uintptr_t)ldsaddr, 16, voff, soff, 0, AUX);
}
DI unsigned mv(unsigned r8) { return ((r8 & 2) << 1) | ((r8 & 4) >> 1); }

template <int S2, class GetBo, class GetBs>
DI void gemm_tile(unsigned aV0, unsigned aV1, unsigned aS0, unsigned aS1, f32x16 (&acc)[4], f32x4 (&as)[2], GetBo getBo, GetBs getBs) {
  constexpr int PD = 3;
  bf16x8 fr[PD + 1], fs[2], bo[2], bs[2];
  static_for<16 + PD>([&](auto rc) {
    constexpr int r = decltype(rc)::value, p = r - PD;
    if constexpr (r < 16) {
      constexpr int t = r >> 2, D = r & 3;
      if constexpr (D == 0) bo[t & 1] = getBo(IntC<t>{});
      const unsigned a = ((D & 1) ? aV1 : aV0) + t * 4096 + (D >> 1) * 1024;
      fr[r % (PD + 1)] = cat(ldstr(a), ldstr(a + 2048));
      if constexpr (D == 1 && (t & 1) == 0) bs[t >> 1] = getBs(IntC<(t >> 1)>{});
      if constexpr (D == 2) {
        const unsigned s = ((t & 1) ? aS1 : aS0) + (t >> 1) * 8192;
        fs[t & 1] = cat(ldstr(s), ldstr(s + S2));
      }
    }
    if constexpr (p >= 0) {
      constexpr int t = p >> 2, D = p & 3;
      acc[D] = mfmaL(fr[p % (PD + 1)], bo[t & 1], acc[D]); PIN(acc[D]);
      if constexpr (D == 2) { as[t & 1] = mfma32(fs[t & 1], bs[t >> 1], as[t & 1]); PIN(as[t & 1]); }
    }
    SB();
  });
}

#define STAMP(idx)

__global__ __launch_bounds__(512, 2) void attn_kernel(const uint16_t* __restrict__ x, const uint16_t* __restrict__ kc,
                                                      const uint16_t* __restrict__ vc, const uint16_t* __restrict__ wq,
                                                      const uint16_t* __restrict__ wo, char* __restrict__ ws,
                                                      uint16_t* __restrict__ out) {
  extern __shared__ __attribute__((aligned(16))) char lds[];
  const int tid = threadIdx.x, lane = tid & 63;
  const int wave = __builtin_amdgcn_readfirstlane(tid >> 6);
  const int bid = blockIdx.x;
  const int h = 4 * (bid & 7) + ((bid >> 3) & 3), b = bid >> 5;
  const unsigned ldsb = (unsigned)(uintptr_t)(LAS char*)lds;

  if (wave == 4 || wave == 5) {
    const bool isw = wave == 4;
    __builtin_amdgcn_s_setprio(3);
    const __amdgpu_buffer_rsrc_t rs = isw ? mk_rsrc(wq, DM * NH * DH * 2) : mk_rsrc(x + (size_t)b * SQ * DM, SQ * DM * 2);
    const unsigned r8 = lane >> 3, sw = ((lane & 7) ^ mv(r8)) << 4;
    const unsigned voff0 = isw ? ((r8 & 3) + 8 * (r8 >> 2)) * 8192u + sw : r8 * 1024u + sw;
    const unsigned voff1 = isw ? voff0 + 128u : (voff0 ^ 16u) + 8192u;
    const int rot = b + 2 * (h & 3);
    auto issue = [&](int st) {
      unsigned dst = ldsb + stg_off(st) + (isw ? 0 : 16384);
      const int i = (st + rot) & 7;
      if (isw) {
        const unsigned src = (unsigned)i * 64u * 8192u + (unsigned)h * 256u;
#pragma unroll 2
        for (int rg = 0; rg < 8; ++rg) {
          const unsigned so = src + (4 * (rg & 1) + 16 * (rg >> 1)) * 8192u;
          dma16<WX_AUX>(rs, dst, voff0, so); dma16<WX_AUX>(rs, dst + 1024, voff1, so); dst += 2048;
        }
      } else {
        unsigned so = (unsigned)i * 128u;
#pragma unroll 3
        for (int m = 0; m < 9; ++m) { dma16<WX_AUX>(rs, dst, voff0, so); dma16<WX_AUX>(rs, dst + 1024, voff1, so); dst += 2048; so += 16384; }
      }
    };
    issue(0);
    issue(1);
    for (int c = 0; c < 8; ++c) {
      if (c == 7) wait_vm<0>();
      else if (isw) wait_vm<16>();
      else wait_vm<18>();
      RAWBAR();
      if (c + 2 <= 7) issue(c + 2);
    }
    return;
  }
  if (wave > 5) {
    const size_t hb = (size_t)(b * NH + h) * SK * DH;
    const __amdgpu_buffer_rsrc_t rk = mk_rsrc(kc + hb, SK * DH * 2), rv = mk_rsrc(vc + hb, SK * DH * 2);
    const unsigned r8 = lane >> 3, half = wave - 6;
    const unsigned voff = r8 * 256u + (((lane & 7) ^ mv(r8)) << 4), voffx = voff ^ 16u;
    __builtin_amdgcn_s_setprio(3);
    unsigned dst = ldsb + half * 8192, src = half * 8192u;
    const unsigned dend = ldsb + half * 8192 + NSLOT * SLOT;
    auto issueK = [&]() {
#pragma unroll
      for (int m = 0; m < 8; ++m) dma16<KV_AUX>(rk, dst + m * 1024, (m & 2) ? voffx : voff, src + (m >> 1) * 2048 + (m & 1) * 128);
      dst += SLOT; if (dst == dend) dst -= NSLOT * SLOT;
    };
    auto issueV = [&]() {
#pragma unroll
      for (int m = 0; m < 8; ++m) dma16<KV_AUX>(rv, dst + m * 1024, voff, src + (m >> 1) * 2048 + (m & 1) * 128);
      dst += SLOT; if (dst == dend) dst -= NSLOT * SLOT;
      src += 16384u;
    };
#define EARLY_KV 0
    if (EARLY_KV >= 1) issueK();
    if (EARLY_KV >= 2) issueV();
    RAWBAR();
    RAWBAR();
    RAWBAR();
    RAWBAR();
    RAWBAR();
    RAWBAR();
    if (EARLY_KV < 1) issueK();
    if (EARLY_KV < 2) issueV();
    issueK();
    RAWBAR(); issueV();
    RAWBAR(); issueK();
    for (int c = 8; c < 34; c += 2) {
      wait_vm<32>(); RAWBAR(); issueV();
      wait_vm<32>(); RAWBAR(); issueK();
    }
    wait_vm<32>(); RAWBAR(); issueV();
    const __amdgpu_buffer_rsrc_t rw = mk_rsrc(wo, NH * DH * DM * 2);
    const unsigned voffw = r8 * 1024u + (((lane & 7) ^ mv(r8)) << 4);
    auto issueW = [&](int i) {
      const unsigned sw = (unsigned)((h >> 2) * 512 + ((h + (i >> 1)) & 3) * 128 + (i & 1) * 64 + half * 32) * 1024u + (h & 3) * 256u;
#pragma unroll
      for (int m = 0; m < 8; ++m) dma16<0>(rw, dst + m * 1024, voffw, sw + (m >> 1) * 8192 + (m & 1) * 128);
      dst += SLOT; if (dst == dend) dst -= NSLOT * SLOT;
    };
    for (int i = 0; i < 5; ++i) { wait_vm<32>(); RAWBAR(); issueW(i); }
    wait_vm<24>(); RAWBAR();
    RAWBAR();
    issueW(5); issueW(6); issueW(7);
    wait_vm<24>(); RAWBAR();
    wait_vm<0>(); RAWBAR();
    return;
  }

  STAMP(0);
  const int w = wave, g = lane >> 4, i16 = lane & 15, hh = lane >> 5, r32 = lane & 31;
  const unsigned w4k = w * 4096;
  unsigned aV[2], aVs[4];
  unsigned aK[4], aKs[2];
  {
    const unsigned q = i16 >> 2, p = i16 & 3;
    const unsigned r8a = 4 * hh + q, r8b = 4 * (g & 1) + q;
#pragma unroll
    for (int d = 0; d < 2; ++d) aV[d] = ldsb + r8a * 128 + (((4 * d + 2 * (g & 1) + (p >> 1)) ^ mv(r8a)) << 4) + 8 * (p & 1);
#pragma unroll
    for (int t = 0; t < 4; ++t) aVs[t] = ldsb + r8b * 128 + (((2 * t + (p >> 1)) ^ mv(r8b)) << 4) + 8 * (p & 1);
  }
  bf16x8 Q[8];
  {
    unsigned xo[4], xs[2];
#pragma unroll
    for (int t = 0; t < 4; ++t) xo[t] = ldsb + 16384 + w4k + (r32 >> 3) * 1024 + (r32 & 7) * 128 + (((2 * t + hh) ^ mv(r32 & 7) ^ ((r32 >> 3) & 1)) << 4);
#pragma unroll
    for (int k = 0; k < 2; ++k) xs[k] = ldsb + 32768 + (i16 >> 3) * 1024 + (i16 & 7) * 128 + (((4 * k + g) ^ mv(i16 & 7) ^ (i16 >> 3)) << 4);
    const unsigned s0 = ((w & 1) ? aVs[2] : aVs[0]) + (w >> 1) * 1024 + (g >> 1) * 4096;
    const unsigned s1 = ((w & 1) ? aVs[3] : aVs[1]) + (w >> 1) * 1024 + (g >> 1) * 4096;
    f32x16 acc[4]; f32x4 as[2];
#pragma unroll
    for (int D = 0; D < 4; ++D) acc[D] = f32x16{};
    as[0] = f32x4{}; as[1] = f32x4{};
    for (int i = 0; i < 8; ++i) {
      const unsigned so = stg_off(i);
      BAR();
      STAMP(1 + i);
      gemm_tile<2048>(aV[0] + so, aV[1] + so, s0 + so, s1 + so, acc, as,
                      [&](auto tc) { return lds128(xo[decltype(tc)::value] + so); },
                      [&](auto kc_) { return lds128(xs[decltype(kc_)::value] + so); });
    }
    static_for<4>([&](auto dc) {
      constexpr int D = decltype(dc)::value;
      Q[2 * D] = pack8_contig32(quad<0>(acc[D]) * QSCALE, quad<1>(acc[D]) * QSCALE);
      Q[2 * D + 1] = pack8_contig32(quad<2>(acc[D]) * QSCALE, quad<3>(acc[D]) * QSCALE);
    });
    *(LAS bf16x8*)(uintptr_t)(ldsb + MISC_OFF + w * 1024 + lane * 16) = pack8_contig16(as[0] * QSCALE, as[1] * QSCALE);
  }
#pragma unroll
  for (int t = 0; t < 4; ++t) aK[t] = ldsb + (r32 >> 3) * 2048 + (r32 & 7) * 128 + (((2 * t + hh) ^ mv(r32 & 7) ^ ((r32 >> 3) & 1)) << 4);
#pragma unroll
  for (int t = 0; t < 2; ++t) aKs[t] = ldsb + w4k + (i16 >> 3) * 2048 + (i16 & 7) * 128 + (((4 * t + g) ^ mv(i16 & 7) ^ (i16 >> 3)) << 4);
#pragma unroll
  for (int t = 0; t < 4; ++t) aVs[t] += w4k + (g >> 1) * 2048 + SLOT;
  aV[0] += SLOT; aV[1] += SLOT;
  unsigned aQs = ldsb + MISC_OFF + lane * 16;
  PIN(aQs);

  f32x16 O[4], nm = {}, Sa, Sb;
  f32x4 Os[8], nms = {}, Sas;
  float l0 = 0.f, l1 = 0.f, ls = 0.f;
#pragma unroll
  for (int D = 0; D < 4; ++D) O[D] = f32x16{};
#pragma unroll
  for (int T = 0; T < 8; ++T) Os[T] = f32x4{};

  auto max16 = [&](const f32x16& S) {
    float a = max3f(S[0], S[1], S[2]), c = max3f(S[3], S[4], S[5]);
    a = max3f(a, S[6], S[7]); c = max3f(c, S[8], S[9]); a = max3f(a, S[10], S[11]); c = max3f(c, S[12], S[13]);
    return max2f(max3f(a, S[14], S[15]), c);
  };
  auto max4 = [&](const f32x4& S) { return max2f(max3f(S[0], S[1], S[2]), S[3]); };
  auto fixL = [&](f32x16& S, bool first) {
    float d = xmax32(max16(S));
    if (!first) d = fmaxf(d, 0.f);
    S -= d; nm -= d;
    if (!first) {
      const float al = ex2(-d);
#pragma unroll
      for (int D = 0; D < 4; ++D) O[D] *= al;
      l0 *= al; l1 *= al;
    }
  };
  auto fixS = [&](bool first) {
    float d = xmax32(xmax16(max4(Sas)));
    if (!first) d = fmaxf(d, 0.f);
    Sas -= d; nms -= d;
    if (!first) {
      const float al = ex2(-d);
#pragma unroll
      for (int T = 0; T < 8; ++T) Os[T] *= al;
      ls *= al;
    }
  };

  constexpr int PD = 3;
  bf16x8 fr[4];
  STAMP(9);
  BAR();
  STAMP(10);
#pragma unroll
  for (int s = 0; s < 8; ++s) Sa = mfmaL(lds128(aK[s & 3] + (s >> 2) * 1024), Q[s], s ? Sa : nm);
#pragma unroll
  for (int s = 0; s < 4; ++s) Sas = mfma32(lds128(aKs[s & 1] + (s >> 1) * 1024), lds128(aQs + s * 1024), s ? Sas : nms);
#pragma unroll
  for (int r = 0; r < PD; ++r) fr[r] = lds128(aK[r & 3] + 8192);
  fixL(Sa, true); fixS(true);
  SB();

  int sk = 0, sv = 1;
  auto body = [&](auto lastc) {
    constexpr bool LAST = decltype(lastc)::value;
    unsigned P[8], Ps[2];
    bf16x8 ksf[2], qsf[2]; bf16x4 vsf[4];
    int mx = 0, my = 0;
    static_for<32>([&](auto pc) {
      constexpr int p = decltype(pc)::value;
      constexpr int r = p + PD, rj = r & 7;
      if constexpr (r < 8) {
        fr[r & 3] = lds128(aK[rj & 3] + 8192 + (rj >> 2) * 1024);
      } else if constexpr (r < 16) {
        if constexpr (r == 8) RAWBAR();
        const unsigned a = aV[rj & 1] + (rj >> 2) * 4096 + ((rj >> 1) & 1) * 1024;
        fr[r & 3] = cat(ldstr(a), ldstr(a + 2048));
        if constexpr (rj >= 4) vsf[rj - 4] = ldstr(aVs[rj - 4]);
      } else if constexpr (r < 24) {
        if constexpr (!LAST) {
          if constexpr (r == 16) {
            RAWBAR();
            const int dk = sk >= NSLOT - 2 ? (2 - NSLOT) * SLOT : 2 * SLOT;
            sk += sk >= NSLOT - 2 ? 2 - NSLOT : 2;
#pragma unroll
            for (int t = 0; t < 4; ++t) aK[t] += dk;
            aKs[0] += dk; aKs[1] += dk;
          }
          fr[r & 3] = lds128(aK[rj & 3] + (rj >> 2) * 1024);
          if constexpr (rj & 1) { ksf[(rj >> 1) & 1] = lds128(aKs[(rj >> 1) & 1] + (rj >> 2) * 1024); qsf[(rj >> 1) & 1] = lds128(aQs + (rj >> 1) * 1024); }
        }
      } else if constexpr (r < 32) {
        const unsigned a = aV[rj & 1] + 8192 + (rj >> 2) * 4096 + ((rj >> 1) & 1) * 1024;
        fr[r & 3] = cat(ldstr(a), ldstr(a + 2048));
        if constexpr (rj < 4) vsf[rj] = ldstr(aVs[rj] + 1024);
      } else {
        if constexpr (!LAST) fr[r & 3] = lds128(aK[rj & 3] + 8192);
      }
      constexpr int j = p & 7;
      if constexpr (p < 8 || (p >= 16 && p < 24)) {
        constexpr bool A = p < 8;
        f32x16& Sn = A ? Sb : Sa; f32x16& So = A ? Sa : Sb;
        if constexpr (A || !LAST) { Sn = mfmaL(fr[p & 3], Q[j], j ? Sn : nm); PIN(Sn); }
        if constexpr (j > 0) { P[j - 1] = cvtpk(So[2 * j - 2], So[2 * j - 1]); PIN(P[j - 1]); }
        So[2 * j] = ex2(So[2 * j]); So[2 * j + 1] = ex2(So[2 * j + 1]); PIN(So);
        if constexpr (!A && !LAST && (j & 1)) { Sas = mfma32(ksf[(j >> 1) & 1], qsf[(j >> 1) & 1], (j >> 1) ? Sas : nms); PIN(Sas); }
      } else {
        constexpr bool A = p < 16;
        f32x16& Sn = A ? Sb : Sa; f32x16& So = A ? Sa : Sb;
        constexpr int t = j >> 2, D = j & 3;
        const u32x4 pw = {P[4 * t], P[4 * t + 1], P[4 * t + 2], P[4 * t + 3]};
        O[D] = mfmaL(fr[p & 3], __builtin_bit_cast(bf16x8, pw), O[D]); PIN(O[D]);
        if constexpr (j == 0) { P[7] = cvtpk(So[14], So[15]); PIN(P[7]); }
        l0 += So[2 * j]; PIN(l0); l1 += So[2 * j + 1]; PIN(l1);
        if constexpr (A && j < 2) { Sas[2 * j] = ex2(Sas[2 * j]); Sas[2 * j + 1] = ex2(Sas[2 * j + 1]); PIN(Sas); }
        if constexpr (A && j == 2) { Ps[0] = cvtpk(Sas[0], Sas[1]); Ps[1] = cvtpk(Sas[2], Sas[3]); PIN(Ps[0]); PIN(Ps[1]); }
        if constexpr (A && j == 3) { float t0 = Sas[0] + Sas[1]; PIN(t0); float t1 = Sas[2] + Sas[3]; PIN(t1); ls += t0 + t1; PIN(ls); }
        if constexpr ((A && j >= 4) || (!A && j < 4)) {
          constexpr int T = A ? j - 4 : j + 4;
          const u32x2 ps = {Ps[0], Ps[1]};
          Os[T] = mfma16(vsf[T & 3], __builtin_bit_cast(bf16x4, ps), Os[T]); PIN(Os[T]);
        }
        if constexpr (A || !LAST) {
          if constexpr (j == 2) { mx = imax3(Sn[0], Sn[1], Sn[2]); my = imax3(Sn[3], Sn[4], Sn[5]); PIN(mx); PIN(my); }
          if constexpr (j == 3) { mx = imax3(mx, Sn[6], Sn[7]); PIN(mx); }
          if constexpr (j == 4) { my = imax3(my, Sn[8], Sn[9]); mx = imax3(mx, Sn[10], Sn[11]); PIN(mx); PIN(my); }
          if constexpr (j == 5) { my = imax3(my, Sn[12], Sn[13]); mx = imax3(mx, Sn[14], Sn[15]); PIN(mx); PIN(my); }
          if constexpr (j == 6) {
            if constexpr (!A) { my = imax3(my, Sas[0], Sas[1]); mx = imax3(mx, Sas[2], Sas[3]); }
            mx = max(mx, my); PIN(mx);
          }
          if constexpr (j == 7) {
            SB();
            if (__builtin_expect(__any(mx > __float_as_int(THR)), 0)) {
              if constexpr (A && !LAST) { PIN(fr[0]); PIN(fr[1]); PIN(fr[2]); PIN(ksf[0]); PIN(qsf[0]); }
              fixL(Sn, false); if constexpr (!A) fixS(false);
            }
          }
        }
        if constexpr (p == 29) {
          const int dv = sv >= NSLOT - 2 ? (2 - NSLOT) * SLOT : 2 * SLOT;
          sv += sv >= NSLOT - 2 ? 2 - NSLOT : 2;
          aV[0] += dv; aV[1] += dv;
#pragma unroll
          for (int t = 0; t < 4; ++t) aVs[t] += dv;
        }
      }
      SB();
    });
  };
  for (int n = 0; n < 15; ++n) { STAMP(11 + n); body(BoolC<false>{}); }
  STAMP(26);
  body(BoolC<true>{});
  STAMP(27);

  const __amdgpu_buffer_rsrc_t rof = mk_rsrc(ws + WS_OF + (size_t)(b * NH + (h & ~3)) * 36 * 1024, 4 * 36 * 1024);
  GAS unsigned* flag1 = (GAS unsigned*)ws;
  GAS unsigned* flag2 = (GAS unsigned*)(ws + 32768);
  int ln = __builtin_amdgcn_mbcnt_hi(~0u, __builtin_amdgcn_mbcnt_lo(~0u, 0u));
  PIN(ln);
  const int g2 = ln >> 4, j16 = ln & 15, h2 = ln >> 5, q32 = ln & 31;
  const unsigned l16 = ln * 16;
  bf16x8 fo[32], fs[16];
  {
    const float rl = __builtin_amdgcn_rcpf(xadd32(l0 + l1));
    static_for<4>([&](auto dc) {
      constexpr int D = decltype(dc)::value;
      fo[2 * D] = pack8(quad<0>(O[D]) * rl, quad<1>(O[D]) * rl);
      fo[2 * D + 1] = pack8(quad<2>(O[D]) * rl, quad<3>(O[D]) * rl);
    });
#pragma unroll
    for (int s = 0; s < 8; ++s)
      __builtin_amdgcn_raw_buffer_store_b128(__builtin_bit_cast(u32x4, fo[s]), rof, l16, ((h & 3) * 36 + w * 8 + s) * 1024, 16);
  }
  {
    const unsigned cb = ldsb + 1 * SLOT;
#pragma unroll
    for (int T = 0; T < 8; ++T) *(LAS f32x4*)(uintptr_t)(cb + (w * 8 + T) * 1024 + l16) = Os[T];
    *(LAS f32x2*)(uintptr_t)(ldsb + MISC_OFF + 4096 + w * 512 + ln * 8) = f32x2{-nms[0], xadd32(xadd16(ls))};
    BAR();
    f32x2 ml[4];
#pragma unroll
    for (int u = 0; u < 4; ++u) ml[u] = *(const LAS f32x2*)(uintptr_t)(ldsb + MISC_OFF + 4096 + u * 512 + ln * 8);
    const float M = max2f(max2f(ml[0][0], ml[1][0]), max2f(ml[2][0], ml[3][0]));
    float f[4], L = 0.f;
#pragma unroll
    for (int u = 0; u < 4; ++u) { f[u] = ex2(ml[u][0] - M); L += f[u] * ml[u][1]; }
    const float rL = __builtin_amdgcn_rcpf(L);
    f32x4 o0 = {0, 0, 0, 0}, o1 = {0, 0, 0, 0};
#pragma unroll
    for (int u = 0; u < 4; ++u) {
      o0 += *(const LAS f32x4*)(uintptr_t)(cb + (u * 8 + 2 * w) * 1024 + l16) * (f[u] * rL);
      o1 += *(const LAS f32x4*)(uintptr_t)(cb + (u * 8 + 2 * w + 1) * 1024 + l16) * (f[u] * rL);
    }
    const bf16x8 os = pack8(o0, o1);
    __builtin_amdgcn_raw_buffer_store_b128(__builtin_bit_cast(u32x4, os), rof, l16, ((h & 3) * 36 + 32 + w) * 1024, 16);
    *(LAS bf16x8*)(uintptr_t)(aQs + w * 1024) = os;
    wait_vm<0>();
    BAR();
    if (w == 0 && ln == 0) __hip_atomic_store(flag1 + (b * NH + h) * 32, 1u, __ATOMIC_RELAXED, __HIP_MEMORY_SCOPE_AGENT);
#pragma unroll
    for (int s = 0; s < 4; ++s) fs[s] = lds128(aQs + s * 1024);
  }
  STAMP(28);
  f32x16 acc[4]; f32x4 as[2];
  {
    const unsigned q = j16 >> 2, p = j16 & 3;
    const unsigned r8a = 4 * h2 + q, r8b = 4 * (g2 & 1) + q;
    const unsigned v0 = ldsb + r8a * 128 + (((2 * (g2 & 1) + (p >> 1)) ^ mv(r8a)) << 4) + 8 * (p & 1);
    const unsigned v1 = ldsb + r8a * 128 + (((4 + 2 * (g2 & 1) + (p >> 1)) ^ mv(r8a)) << 4) + 8 * (p & 1);
    const unsigned sb = ldsb + r8b * 128 + 8 * (p & 1) + (w >> 1) * 1024 + (g2 >> 1) * 2048;
    const unsigned s0 = sb + (((4 * (w & 1) + (p >> 1)) ^ mv(r8b)) << 4), s1 = sb + (((4 * (w & 1) + 2 + (p >> 1)) ^ mv(r8b)) << 4);
#pragma unroll
    for (int D = 0; D < 4; ++D) acc[D] = f32x16{};
    as[0] = f32x4{}; as[1] = f32x4{};
    auto ldB = [&](int i) {
      const unsigned hb = ((h + (i >> 1)) & 3) * 36;
#pragma unroll
      for (int t = 0; t < 4; ++t)
        fo[i * 4 + t] = __builtin_bit_cast(bf16x8, __builtin_amdgcn_raw_buffer_load_b128(rof, l16, (hb + w * 8 + (i & 1) * 4 + t) * 1024, 16));
#pragma unroll
      for (int k = 0; k < 2; ++k)
        fs[i * 2 + k] = __builtin_bit_cast(bf16x8, __builtin_amdgcn_raw_buffer_load_b128(rof, l16, (hb + 32 + (i & 1) * 2 + k) * 1024, 16));
    };
    static_for<8>([&](auto ic) {
      constexpr int i = decltype(ic)::value;
      if constexpr (i == 1) {
        GAS unsigned* fp = flag1 + (b * NH + (h & ~3) + (ln & 3)) * 32;
        wait_flags(fp);
        __builtin_amdgcn_fence(__ATOMIC_ACQUIRE, "wavefront");
        STAMP(29);
        ldB(2); ldB(3); ldB(4); ldB(5);
      }
      if constexpr (i == 2 || i == 5) RAWBAR();
      if constexpr (i == 2) ldB(6);
      if constexpr (i == 3) ldB(7);
      unsigned so = ((32 + i) % NSLOT) * SLOT;
      asm volatile("" : "+s"(so));
      gemm_tile<4096>(v0 + so, v1 + so, s0 + so, s1 + so, acc, as,
                      [&](auto tc) { return fo[i * 4 + decltype(tc)::value]; }, [&](auto kc_) { return fs[i * 2 + decltype(kc_)::value]; });
    });
  }
  STAMP(30);
  {
    const int hg = h >> 2, nt = h & 3;
    const __amdgpu_buffer_rsrc_t rp = mk_rsrc(ws + WS_PART + (size_t)(b * 4 + nt) * 8 * 72 * 1024, 8 * 72 * 1024);
    static_for<4>([&](auto dc) {
      constexpr int D = decltype(dc)::value;
      const unsigned o = (hg * 72 + w * 16 + D * 4) * 1024;
      __builtin_amdgcn_raw_buffer_store_b128(__builtin_bit_cast(u32x4, quad<0>(acc[D])), rp, l16, o, 16);
      __builtin_amdgcn_raw_buffer_store_b128(__builtin_bit_cast(u32x4, quad<1>(acc[D])), rp, l16, o + 1024, 16);
      __builtin_amdgcn_raw_buffer_store_b128(__builtin_bit_cast(u32x4, quad<2>(acc[D])), rp, l16, o + 2048, 16);
      __builtin_amdgcn_raw_buffer_store_b128(__builtin_bit_cast(u32x4, quad<3>(acc[D])), rp, l16, o + 3072, 16);
    });
    __builtin_amdgcn_raw_buffer_store_b128(__builtin_bit_cast(u32x4, as[0]), rp, l16, (hg * 72 + 64 + 2 * w) * 1024, 16);
    __builtin_amdgcn_raw_buffer_store_b128(__builtin_bit_cast(u32x4, as[1]), rp, l16, (hg * 72 + 65 + 2 * w) * 1024, 16);
    wait_vm<0>();
    RAWBAR();
    if (w == 0 && ln == 0) __hip_atomic_store(flag2 + (b * NH + h) * 32, 1u, __ATOMIC_RELAXED, __HIP_MEMORY_SCOPE_AGENT);
    STAMP(31);
    GAS unsigned* fp = flag2 + (b * NH + (ln & 7) * 4 + nt) * 32;
    wait_flags(fp);
    __builtin_amdgcn_fence(__ATOMIC_ACQUIRE, "wavefront");
    STAMP(32);
    const int nblk = w == 0 ? 3 : 2;
    f32x4 v[3][8];
#pragma unroll
    for (int j = 0; j < 3; ++j)
      if (j < nblk) {
#pragma unroll
        for (int u = 0; u < 8; ++u)
          v[j][u] = __builtin_bit_cast(f32x4, __builtin_amdgcn_raw_buffer_load_b128(rp, l16, (u * 72 + hg * 9 + w + 4 * j) * 1024, 16));
      }
#pragma unroll
    for (int j = 0; j < 3; ++j)
      if (j < nblk) {
        const f32x4 sum = ((v[j][0] + v[j][1]) + (v[j][2] + v[j][3])) + ((v[j][4] + v[j][5]) + (v[j][6] + v[j][7]));
        const int k = hg * 9 + w + 4 * j;
        const int row = k < 64 ? (k >> 4) * 32 + q32 : 128 + j16;
        const int col = k < 64 ? (k & 15) * 8 + 4 * h2 : (k - 64) * 16 + 4 * g2;
        const u32x2 o = {cvtpk(sum[0], sum[1]), cvtpk(sum[2], sum[3])};
        *(u32x2*)(out + ((size_t)(b * SQ + row) * DM + nt * 128 + col)) = o;
      }
  }
  STAMP(33);
}

extern "C" void kernel_launch(void* const* d_in, const int* in_sizes, int n_in, void* d_out, int out_size, void* d_ws,
                              size_t ws_size, hipStream_t stream) {
  const uint16_t* x = (const uint16_t*)d_in[0];
  const uint16_t* kc = (const uint16_t*)d_in[1];
  const uint16_t* vc = (const uint16_t*)d_in[2];
  const uint16_t* wq = (const uint16_t*)d_in[3];
  const uint16_t* wo = (const uint16_t*)d_in[4];
  (void)hipFuncSetAttribute((const void*)attn_kernel, hipFuncAttributeMaxDynamicSharedMemorySize, LDS_BYTES);
  (void)hipMemsetAsync(d_ws, 0, WS_FLAG_BYTES, stream);
  hipLaunchKernelGGL(attn_kernel, dim3(256), dim3(512), LDS_BYTES, stream, x, kc, vc, wq, wo, (char*)d_ws, (uint16_t*)d_out);
}
```

```cpp
#include <hip/hip_runtime.h>
#include <hip/hip_bf16.h>
#include <stdint.h>
#include <utility>

typedef __bf16 bf16x8 __attribute__((ext_vector_type(8)));
typedef __bf16 bf16x4 __attribute__((ext_vector_type(4)));
typedef __bf16 bf16x2 __attribute__((ext_vector_type(2)));
typedef short s16x4 __attribute__((ext_vector_type(4)));
typedef float f32x16 __attribute__((ext_vector_type(16)));
typedef float f32x4 __attribute__((ext_vector_type(4)));
typedef float f32x2 __attribute__((ext_vector_type(2)));
typedef unsigned u32x4 __attribute__((ext_vector_type(4)));
typedef unsigned u32x2 __attribute__((ext_vector_type(2)));
#define LAS __attribute__((address_space(3)))
#define GAS __attribute__((address_space(1)))
#define DI __device__ __forceinline__

constexpr int NB = 8, SQ = 144, DM = 512, NH = 32, SK = 1024, DH = 128;
constexpr int SLOT = 16384, NSLOT = 6, LDS_BYTES = 163840;
constexpr int STG0 = 3 * SLOT, STG = 34 * 1024, MISC_OFF = STG0 + 3 * STG;
constexpr int XCH_OFF = MISC_OFF;
constexpr int ML_OFF = MISC_OFF + 8192;
constexpr int CTL_OFF = MISC_OFF + 9216;
DI int stg_off(int i) { return STG0 + ((i + 1) % 3) * STG; }
#define KV_AUX 2
#define WO_AUX 0
#define WX_AUX 0
constexpr size_t WS_FLAG_BYTES = 65536, WS_OF = WS_FLAG_BYTES, WS_PART = WS_OF + (size_t)256 * 36 * 1024, WS_END = WS_PART + (size_t)256 * 36 * 1024;
constexpr int SPIN_CAP = 1 << 16;
constexpr float QSCALE = 0.088388347648318440f * 1.4426950408889634f;
constexpr float THR = 8.0f;

DI f32x16 mfmaL(bf16x8 a, bf16x8 b, f32x16 c) { return __builtin_amdgcn_mfma_f32_32x32x16_bf16(a, b, c, 0, 0, 0); }
DI f32x4 mfma32(bf16x8 a, bf16x8 b, f32x4 c) { return __builtin_amdgcn_mfma_f32_16x16x32_bf16(a, b, c, 0, 0, 0); }
DI f32x4 mfma16(bf16x4 a, bf16x4 b, f32x4 c) {
  return __builtin_amdgcn_mfma_f32_16x16x16bf16_1k(__builtin_bit_cast(s16x4, a), __builtin_bit_cast(s16x4, b), c, 0, 0, 0);
}
DI unsigned cvtpk(float lo, float hi) { f32x2 v = {lo, hi}; bf16x2 b = __builtin_convertvector(v, bf16x2); return __builtin_bit_cast(unsigned, b); }
DI float max3f(float a, float b, float c) { return fmaxf(fmaxf(a, b), c); }
DI float max2f(float a, float b) { return fmaxf(a, b); }
DI int imax3(float a, float b, float c) { return max(max(__float_as_int(a), __float_as_int(b)), __float_as_int(c)); }
DI int imax3(int a, float b, float c) { return max(max(a, __float_as_int(b)), __float_as_int(c)); }
DI float ex2(float x) { return __builtin_amdgcn_exp2f(x); }
DI bf16x8 lds128(unsigned addr) { return __builtin_bit_cast(bf16x8, *(const volatile LAS u32x4*)(uintptr_t)addr); }
DI bf16x4 ldstr(unsigned addr) { return __builtin_amdgcn_ds_read_tr16_b64_v4bf16((LAS bf16x4*)(uintptr_t)addr); }
DI bf16x8 cat(bf16x4 a, bf16x4 b) { return __builtin_shufflevector(a, b, 0, 1, 2, 3, 4, 5, 6, 7); }
#define BAR() asm volatile("s_waitcnt lgkmcnt(0)\n\ts_barrier" ::: "memory")
#define RAWBAR() asm volatile("s_barrier" ::: "memory")
#define SB() __builtin_amdgcn_sched_barrier(0)
#define PIN(x) asm volatile("" : "+v"(x))
template <int I> struct IntC { static constexpr int value = I; };
template <bool V> struct BoolC { static constexpr bool value = V; };
template <class F, int... Is> DI void static_for_(F&& f, std::integer_sequence<int, Is...>) { (f(IntC<Is>{}), ...); }
template <int N, class F> DI void static_for(F&& f) { static_for_(f, std::make_integer_sequence<int, N>{}); }
DI float xmax32(float x) {
  auto b = __builtin_amdgcn_permlane32_swap(__float_as_uint(x), __float_as_uint(x), false, false);
  return max2f(__uint_as_float(b[0]), __uint_as_float(b[1]));
}
DI float xmax16(float x) {
  auto a = __builtin_amdgcn_permlane16_swap(__float_as_uint(x), __float_as_uint(x), false, false);
  return max2f(__uint_as_float(a[0]), __uint_as_float(a[1]));
}
DI float xadd32(float x) {
  auto b = __builtin_amdgcn_permlane32_swap(__float_as_uint(x), __float_as_uint(x), false, false);
  return __uint_as_float(b[0]) + __uint_as_float(b[1]);
}
DI float xadd16(float x) {
  auto b = __builtin_amdgcn_permlane16_swap(__float_as_uint(x), __float_as_uint(x), false, false);
  return __uint_as_float(b[0]) + __uint_as_float(b[1]);
}
DI bf16x8 pack8(f32x4 a, f32x4 b) {
  u32x4 w = {cvtpk(a[0], a[1]), cvtpk(a[2], a[3]), cvtpk(b[0], b[1]), cvtpk(b[2], b[3])};
  return __builtin_bit_cast(bf16x8, w);
}
template <int Q> DI f32x4 quad(const f32x16& v) { return f32x4{v[4 * Q], v[4 * Q + 1], v[4 * Q + 2], v[4 * Q + 3]}; }
DI bf16x8 pack8_contig16(f32x4 x, f32x4 y) {
  unsigned x0 = cvtpk(x[0], x[1]), x1 = cvtpk(x[2], x[3]), y0 = cvtpk(y[0], y[1]), y1 = cvtpk(y[2], y[3]);
  auto a0 = __builtin_amdgcn_permlane32_swap(x0, y0, false, false);
  auto a1 = __builtin_amdgcn_permlane32_swap(x1, y1, false, false);
  auto b0 = __builtin_amdgcn_permlane16_swap(a0[0], a0[1], false, false);
  auto b1 = __builtin_amdgcn_permlane16_swap(a1[0], a1[1], false, false);
  u32x4 w = {b0[0], b1[0], b0[1], b1[1]};
  return __builtin_bit_cast(bf16x8, w);
}
DI bf16x8 pack8_contig32(f32x4 x, f32x4 y) {
  unsigned x0 = cvtpk(x[0], x[1]), x1 = cvtpk(x[2], x[3]), y0 = cvtpk(y[0], y[1]), y1 = cvtpk(y[2], y[3]);
  auto a0 = __builtin_amdgcn_permlane32_swap(x0, y0, false, false);
  auto a1 = __builtin_amdgcn_permlane32_swap(x1, y1, false, false);
  u32x4 w = {a0[0], a1[0], a0[1], a1[1]};
  return __builtin_bit_cast(bf16x8, w);
}
template <int N> DI void wait_vm() { asm volatile("s_waitcnt vmcnt(%0)" ::"i"(N) : "memory"); }
DI void wait_flags(GAS unsigned* fp) {
  for (int it = 0; it < SPIN_CAP; ++it) {
    if (__all(__hip_atomic_load(fp, __ATOMIC_RELAXED, __HIP_MEMORY_SCOPE_AGENT) == 1u)) break;
    __builtin_amdgcn_s_sleep(1);
  }
}
DI __amdgpu_buffer_rsrc_t mk_rsrc(const void* p, unsigned bytes) { return __builtin_amdgcn_make_buffer_rsrc((void*)p, 0, bytes, 0x00020000); }
template <int AUX> DI void dma16(__amdgpu_buffer_rsrc_t r, unsigned ldsaddr, unsigned voff, unsigned soff) {
  __builtin_amdgcn_raw_ptr_buffer_load_lds(r, (LAS void*)(uintptr_t)ldsaddr, 16, voff, soff, 0, AUX);
}
DI unsigned mv(unsigned r8) { return ((r8 & 2) << 1) | ((r8 & 4) >> 1); }

DI unsigned ldsw(unsigned addr) { return *(const volatile LAS unsigned*)(uintptr_t)addr; }
DI u32x2 ldsw2(unsigned addr) { return *(const volatile LAS u32x2*)(uintptr_t)addr; }
DI u32x4 ldsw4(unsigned addr) { return *(const volatile LAS u32x4*)(uintptr_t)addr; }
DI void stsw(unsigned addr, unsigned v) { *(volatile LAS unsigned*)(uintptr_t)addr = v; }
#define MEMFENCE() asm volatile("" ::: "memory")
DI void wait2(unsigned addr, unsigned need) {
  for (int it = 0; it < SPIN_CAP; ++it) {
    const u32x2 v = ldsw2(addr);
    if ((unsigned)__builtin_amdgcn_readfirstlane(min(v[0], v[1])) >= need) break;
    __builtin_amdgcn_s_sleep(1);
  }
  MEMFENCE();
}
DI void wait1(unsigned addr, unsigned need) {
  for (int it = 0; it < SPIN_CAP; ++it) {
    if ((unsigned)__builtin_amdgcn_readfirstlane(ldsw(addr)) >= need) break;
    __builtin_amdgcn_s_sleep(1);
  }
  MEMFENCE();
}
DI unsigned done_min(unsigned addr) {
  const u32x4 a = ldsw4(addr); const u32x2 c = ldsw2(addr + 16);
  return __builtin_amdgcn_readfirstlane(min(min(min(a[0], a[1]), min(a[2], a[3])), min(c[0], c[1])));
}
DI void set_flag(GAS unsigned* p) { __hip_atomic_store(p, 1u, __ATOMIC_RELAXED, __HIP_MEMORY_SCOPE_AGENT); }
DI unsigned wo_slot(int i) { return (i < 3 ? 6 + i : (32 + i - 3) % NSLOT) * SLOT; }

#define STAMP(idx)

DI f32x4 bf_lo(u32x4 v) { return f32x4{__uint_as_float(v[0] << 16), __uint_as_float(v[0] & 0xffff0000u), __uint_as_float(v[1] << 16), __uint_as_float(v[1] & 0xffff0000u)}; }
DI f32x4 bf_hi(u32x4 v) { return f32x4{__uint_as_float(v[2] << 16), __uint_as_float(v[2] & 0xffff0000u), __uint_as_float(v[3] << 16), __uint_as_float(v[3] & 0xffff0000u)}; }
DI void reduce_blocks(char* ws, uint16_t* out, int b, int h, int L, int lane) {
  const int hg = h >> 2, nt = h & 3;
  const __amdgpu_buffer_rsrc_t rp = mk_rsrc(ws + WS_PART + (size_t)(b * 4 + nt) * 8 * 36 * 1024, 8 * 36 * 1024);
  GAS unsigned* flag2 = (GAS unsigned*)(ws + 32768) + (b * 4 + nt) * 256;
  __builtin_amdgcn_s_setprio(0);
  const int nb = hg < 2 ? 3 : 2;
  for (int itb = 0; itb < nb; ++itb) {
    const int it = itb == 2, r = 2 * L + (itb & 1);
    const int wp = 2 * hg + L;
    const int blk = it ? 32 + wp : r * 8 + hg;
    wait_flags(flag2 + (it ? 128 + (wp >> 1) * 8 : (r * 4 + (hg >> 1)) * 8) + (lane & 7));
    __builtin_amdgcn_fence(__ATOMIC_ACQUIRE, "wavefront");
    u32x4 v[8];
#pragma unroll
    for (int u = 0; u < 8; ++u) v[u] = __builtin_amdgcn_raw_buffer_load_b128(rp, lane * 16, (u * 36 + blk) * 1024, 16);
    const f32x4 x = ((bf_lo(v[0]) + bf_lo(v[1])) + (bf_lo(v[2]) + bf_lo(v[3]))) + ((bf_lo(v[4]) + bf_lo(v[5])) + (bf_lo(v[6]) + bf_lo(v[7])));
    const f32x4 y = ((bf_hi(v[0]) + bf_hi(v[1])) + (bf_hi(v[2]) + bf_hi(v[3]))) + ((bf_hi(v[4]) + bf_hi(v[5])) + (bf_hi(v[6]) + bf_hi(v[7])));
    if (it == 0) {
      const bf16x8 o = pack8_contig32(x, y);
      *(u32x4*)(out + ((size_t)(b * SQ + r * 32 + (lane & 31)) * DM + nt * 128 + hg * 16 + (lane >> 5) * 8)) = __builtin_bit_cast(u32x4, o);
    } else {
      const bf16x8 o = pack8_contig16(x, y);
      *(u32x4*)(out + ((size_t)(b * SQ + 128 + (lane & 15)) * DM + nt * 128 + wp * 32 + (lane >> 4) * 8)) = __builtin_bit_cast(u32x4, o);
    }
  }
}

__global__ __launch_bounds__(512, 2) void attn_kernel(const uint16_t* __restrict__ x, const uint16_t* __restrict__ kc,
                                                      const uint16_t* __restrict__ vc, const uint16_t* __restrict__ wq,
                                                      const uint16_t* __restrict__ wo, char* __restrict__ ws,
                                                      uint16_t* __restrict__ out) {
  extern __shared__ __attribute__((aligned(16))) char lds[];
  const int tid = threadIdx.x, lane = tid & 63;
  const int wave = __builtin_amdgcn_readfirstlane(tid >> 6);
  const int bid = blockIdx.x;
  const int h = 4 * (bid & 7) + ((bid >> 3) & 3), b = bid >> 5;
  const unsigned ldsb = (unsigned)(uintptr_t)(LAS char*)lds;
  const unsigned aFULL = ldsb + CTL_OFF, aWOF = aFULL + 8, aDONE = aFULL + 16, aPAIR = aFULL + 48;
  const __amdgpu_buffer_rsrc_t rof = mk_rsrc(ws + WS_OF + (size_t)(b * NH + (h & ~3)) * 36 * 1024, 4 * 36 * 1024);
  GAS unsigned* flag1 = (GAS unsigned*)ws + (b * NH + (h & ~3)) * 32;
  GAS unsigned* flag2 = (GAS unsigned*)(ws + 32768) + (b * 4 + (h & 3)) * 256;
  const __amdgpu_buffer_rsrc_t rp = mk_rsrc(ws + WS_PART + (size_t)((b * 4 + (h & 3)) * 8 + (h >> 2)) * 36 * 1024, 36 * 1024);

  if (wave >= 6) {
    __builtin_amdgcn_s_setprio(3);
    const unsigned half = wave - 6, r8 = lane >> 3, sw = ((lane & 7) ^ mv(r8)) << 4;
    const unsigned aMy = aFULL + half * 4, aMyW = aWOF + half * 4;
    {
      const bool isw = half == 0;
      const __amdgpu_buffer_rsrc_t rs = isw ? mk_rsrc(wq, DM * NH * DH * 2) : mk_rsrc(x + (size_t)b * SQ * DM, SQ * DM * 2);
      const unsigned voff0 = isw ? ((r8 & 3) + 8 * (r8 >> 2)) * 8192u + sw : r8 * 1024u + sw;
      const unsigned voff1 = isw ? voff0 + 128u : (voff0 ^ 16u) + 8192u;
      const int rot = b + 2 * (h & 3);
      auto issue = [&](int st) {
        unsigned dst = ldsb + stg_off(st) + (isw ? 0 : 16384);
        const int i = (st + rot) & 7;
        if (isw) {
          const unsigned src = (unsigned)i * 64u * 8192u + (unsigned)h * 256u;
#pragma unroll 2
          for (int rg = 0; rg < 8; ++rg) {
            const unsigned so = src + (4 * (rg & 1) + 16 * (rg >> 1)) * 8192u;
            dma16<WX_AUX>(rs, dst, voff0, so); dma16<WX_AUX>(rs, dst + 1024, voff1, so); dst += 2048;
          }
        } else {
          unsigned so = (unsigned)i * 128u;
#pragma unroll 3
          for (int m = 0; m < 9; ++m) { dma16<WX_AUX>(rs, dst, voff0, so); dma16<WX_AUX>(rs, dst + 1024, voff1, so); dst += 2048; so += 16384; }
        }
      };
      issue(0);
      BAR();
      for (int st = 1; st < 8; ++st) {
        if (st >= 3 && done_min(aDONE) < (unsigned)(st - 2)) {
          wait_vm<0>(); stsw(aMy, st);
          for (int it = 0; it < SPIN_CAP && done_min(aDONE) < (unsigned)(st - 2); ++it) __builtin_amdgcn_s_sleep(1);
        }
        MEMFENCE();
        issue(st);
        if (isw) wait_vm<16>(); else wait_vm<18>();
        stsw(aMy, st);
      }
    }
    const size_t hb = (size_t)(b * NH + h) * SK * DH;
    const __amdgpu_buffer_rsrc_t rk = mk_rsrc(kc + hb, SK * DH * 2), rv = mk_rsrc(vc + hb, SK * DH * 2), rw = mk_rsrc(wo, NH * DH * DM * 2);
    const unsigned voff = r8 * 256u + sw, voffx = voff ^ 16u, voffw = r8 * 1024u + sw;
    unsigned p1a = aMy, p1v = 8, p2a = aMy, p2v = 8, p3a = aMy, p3v = 8;
    int j = 0, wi = 0, slot = 0;
    for (int n = 0; n < 40; ++n) {
      const bool isW = (n == 8 || n == 13 || n == 18 || n >= 35);
      unsigned need;
      if (isW) need = wi < 3 ? 8u : (unsigned)(8 + 32 + wi - 3 - NSLOT + 1);
      else need = j < 3 ? 0u : (j < 5 ? 6u : (j < 6 ? 7u : (unsigned)(8 + j - NSLOT + 1)));
      if (done_min(aDONE) < need) {
        wait_vm<0>(); stsw(p3a, p3v); stsw(p2a, p2v); stsw(p1a, p1v);
        p3a = p2a = p1a; p3v = p2v = p1v;
        for (int it = 0; it < SPIN_CAP && done_min(aDONE) < need; ++it) __builtin_amdgcn_s_sleep(1);
      }
      MEMFENCE();
      unsigned na, nv;
      if (isW) {
        const unsigned dst = ldsb + (wi < 3 ? 6 + wi : slot) * SLOT + half * 8192;
        const unsigned src = (unsigned)((h >> 2) * 512 + ((h + (wi >> 1)) & 3) * 128 + (wi & 1) * 64 + half * 32) * 1024u + (h & 3) * 256u;
#pragma unroll
        for (int m = 0; m < 8; ++m) dma16<WO_AUX>(rw, dst + m * 1024, voffw, src + (m >> 1) * 8192 + (m & 1) * 128);
        if (wi >= 3 && ++slot == NSLOT) slot = 0;
        ++wi; na = aMyW; nv = wi;
      } else {
        const unsigned dst = ldsb + slot * SLOT + half * 8192, src = (unsigned)(j >> 1) * 16384u + half * 8192u;
        if (j & 1) {
#pragma unroll
          for (int m = 0; m < 8; ++m) dma16<KV_AUX>(rv, dst + m * 1024, voff, src + (m >> 1) * 2048 + (m & 1) * 128);
        } else {
#pragma unroll
          for (int m = 0; m < 8; ++m) dma16<KV_AUX>(rk, dst + m * 1024, (m & 2) ? voffx : voff, src + (m >> 1) * 2048 + (m & 1) * 128);
        }
        if (++slot == NSLOT) slot = 0;
        ++j; na = aMy; nv = 8 + j;
      }
      if (n == 0) { wait_vm<8>(); stsw(aMy, 8); }
      else { wait_vm<24>(); stsw(p3a, p3v); }
      p3a = p2a; p3v = p2v; p2a = p1a; p2v = p1v; p1a = na; p1v = nv;
    }
    wait_vm<16>(); stsw(p3a, p3v);
    wait_vm<8>(); stsw(p2a, p2v);
    wait_vm<0>(); stsw(p1a, p1v);
    reduce_blocks(ws, out, b, h, wave - 6, lane);
    STAMP(35);
    return;
  }

  if (wave == 0 && lane < 16) stsw(aFULL + lane * 4, 0);
  BAR();
  STAMP(0);
  const int g = lane >> 4, i16 = lane & 15;

  if (wave >= 4) {
    const int e = wave - 4;
    const unsigned aMyDone = aDONE + wave * 4;
    const unsigned q = i16 >> 2, p = i16 & 3, r8b = 4 * (g & 1) + q;
    unsigned aT[4];
#pragma unroll
    for (int t = 0; t < 4; ++t) aT[t] = ldsb + r8b * 128 + (((2 * t + (p >> 1)) ^ mv(r8b)) << 4) + 8 * (p & 1);
    unsigned aR[2];
#pragma unroll
    for (int t = 0; t < 2; ++t) aR[t] = (i16 & 7) * 128 + (((4 * t + g) ^ mv(i16 & 7) ^ (i16 >> 3)) << 4);
    bf16x8 Q[4];
    {
      f32x4 a[4] = {};
      for (int i = 0; i < 8; ++i) {
        wait2(aFULL, i + 1);
        const unsigned so = stg_off(i);
        bf16x8 xb[2], wf[8];
#pragma unroll
        for (int kk = 0; kk < 2; ++kk) {
          xb[kk] = lds128(ldsb + so + 32768 + (i16 >> 3) * 1024 + aR[kk]);
#pragma unroll
          for (int t = 0; t < 4; ++t) {
            const unsigned ad = aT[t] + so + e * 1024 + (g >> 1) * 4096 + kk * 8192;
            wf[kk * 4 + t] = cat(ldstr(ad), ldstr(ad + 2048));
          }
        }
        MEMFENCE();
        stsw(aMyDone, i + 1);
        SB();
#pragma unroll
        for (int k = 0; k < 8; ++k) a[k & 3] = mfma32(wf[k], xb[k >> 2], a[k & 3]);
      }
      *(LAS bf16x8*)(uintptr_t)(ldsb + XCH_OFF + (2 * e) * 1024 + lane * 16) = pack8_contig16(a[0] * QSCALE, a[1] * QSCALE);
      *(LAS bf16x8*)(uintptr_t)(ldsb + XCH_OFF + (2 * e + 1) * 1024 + lane * 16) = pack8_contig16(a[2] * QSCALE, a[3] * QSCALE);
      MEMFENCE();
      stsw(aPAIR + e * 4, 1);
      wait2(aPAIR, 1);
#pragma unroll
      for (int s = 0; s < 4; ++s) Q[s] = lds128(ldsb + XCH_OFF + s * 1024 + lane * 16);
    }
    STAMP(9);
    f32x4 Os[8] = {}; float nm = 0.f, ls = 0.f;
    int slot = 0;
    for (int n = 0; n < 16; ++n) {
      wait2(aFULL, 9 + 2 * n);
      const unsigned sk = ldsb + slot * SLOT + e * 8192 + (i16 >> 3) * 2048;
      f32x4 S0 = {nm, nm, nm, nm}, S1 = S0;
      bf16x8 kf[8];
#pragma unroll
      for (int s = 0; s < 4; ++s) { kf[s] = lds128(sk + aR[s & 1] + (s >> 1) * 1024); kf[4 + s] = lds128(sk + 4096 + aR[s & 1] + (s >> 1) * 1024); }
      MEMFENCE();
      stsw(aMyDone, 9 + 2 * n);
      SB();
#pragma unroll
      for (int s = 0; s < 4; ++s) { S0 = mfma32(kf[s], Q[s], S0); S1 = mfma32(kf[4 + s], Q[s], S1); }
      const float mx = max2f(max2f(max2f(S0[0], S0[1]), max2f(S0[2], S0[3])), max2f(max2f(S1[0], S1[1]), max2f(S1[2], S1[3])));
      if (n == 0 || __any(mx > THR)) {
        float d = xmax32(xmax16(mx));
        if (n) d = fmaxf(d, 0.f);
        S0 -= d; S1 -= d; nm -= d;
        const float al = n ? ex2(-d) : 0.f;
#pragma unroll
        for (int T = 0; T < 8; ++T) Os[T] *= al;
        ls *= al;
      }
#pragma unroll
      for (int r = 0; r < 4; ++r) { S0[r] = ex2(S0[r]); S1[r] = ex2(S1[r]); }
      ls += ((S0[0] + S0[1]) + (S0[2] + S0[3])) + ((S1[0] + S1[1]) + (S1[2] + S1[3]));
      const bf16x8 P = pack8(S0, S1);
      wait2(aFULL, 10 + 2 * n);
      const unsigned sv = (slot + 1) * SLOT + e * 8192 + (g >> 1) * 2048;
#pragma unroll
      for (int T = 0; T < 8; ++T) {
        const unsigned ad = aT[T & 3] + sv + (T >> 2) * 1024;
        kf[T] = cat(ldstr(ad), ldstr(ad + 4096));
      }
      MEMFENCE();
      stsw(aMyDone, 10 + 2 * n);
      SB();
#pragma unroll
      for (int T = 0; T < 8; ++T) Os[T] = mfma32(kf[T], P, Os[T]);
      slot += 2; if (slot == NSLOT) slot = 0;
    }
    STAMP(27);
    bf16x8 fs[16];
    {
      ls = xadd32(xadd16(ls));
#pragma unroll
      for (int t = 0; t < 4; ++t) *(LAS f32x4*)(uintptr_t)(ldsb + XCH_OFF + (e * 4 + t) * 1024 + lane * 16) = e ? Os[t] : Os[4 + t];
      *(LAS f32x2*)(uintptr_t)(ldsb + ML_OFF + e * 512 + lane * 8) = f32x2{-nm, ls};
      MEMFENCE();
      stsw(aPAIR + e * 4, 2);
      wait2(aPAIR, 2);
      const f32x2 ml = *(const LAS f32x2*)(uintptr_t)(ldsb + ML_OFF + (1 - e) * 512 + lane * 8);
      const float M = max2f(-nm, ml[0]);
      const float f0 = ex2(-nm - M), f1 = ex2(ml[0] - M);
      const float rL = __builtin_amdgcn_rcpf(f0 * ls + f1 * ml[1]);
      f32x4 o[4];
#pragma unroll
      for (int t = 0; t < 4; ++t)
        o[t] = (e ? Os[4 + t] : Os[t]) * (f0 * rL) + *(const LAS f32x4*)(uintptr_t)(ldsb + XCH_OFF + ((1 - e) * 4 + t) * 1024 + lane * 16) * (f1 * rL);
      const bf16x8 o0 = pack8(o[0], o[1]), o1 = pack8(o[2], o[3]);
      __builtin_amdgcn_raw_buffer_store_b128(__builtin_bit_cast(u32x4, o0), rof, lane * 16, ((h & 3) * 36 + 32 + 2 * e) * 1024, 16);
      __builtin_amdgcn_raw_buffer_store_b128(__builtin_bit_cast(u32x4, o1), rof, lane * 16, ((h & 3) * 36 + 33 + 2 * e) * 1024, 16);
      wait_vm<0>();
      if (lane == 0) set_flag(flag1 + (h & 3) * 32 + 4 + e);
    }
    wait_flags(flag1 + (lane & 3) * 32 + 4 + ((lane >> 2) & 1));
    __builtin_amdgcn_fence(__ATOMIC_ACQUIRE, "wavefront");
#pragma unroll
    for (int k = 0; k < 16; ++k)
      fs[k] = __builtin_bit_cast(bf16x8, __builtin_amdgcn_raw_buffer_load_b128(rof, lane * 16, (((h + (k >> 2)) & 3) * 36 + 32 + (k & 3)) * 1024, 16));
    wait2(aWOF, 8);
    {
      f32x4 a[4] = {};
      bf16x8 wf[2][8];
      auto rdw = [&](int i) {
        unsigned so = wo_slot(i) + e * 1024;
        asm volatile("" : "+s"(so));
#pragma unroll
        for (int k = 0; k < 8; ++k) {
          const unsigned ad = aT[k & 3] + so + (g >> 1) * 2048 + (k >> 2) * 8192;
          wf[i & 1][k] = cat(ldstr(ad), ldstr(ad + 4096));
        }
      };
      rdw(0);
#pragma unroll
      for (int i = 0; i < 8; ++i) {
        if (i < 7) rdw(i + 1);
        SB();
#pragma unroll
        for (int k = 0; k < 8; ++k) a[k & 3] = mfma32(wf[i & 1][k], fs[i * 2 + (k >> 2)], a[k & 3]);
        PIN(a[0]); PIN(a[1]); PIN(a[2]); PIN(a[3]);
        SB();
      }
      __builtin_amdgcn_raw_buffer_store_b128(__builtin_bit_cast(u32x4, pack8(a[0], a[1])), rp, lane * 16, (32 + 2 * e) * 1024, 16);
      __builtin_amdgcn_raw_buffer_store_b128(__builtin_bit_cast(u32x4, pack8(a[2], a[3])), rp, lane * 16, (33 + 2 * e) * 1024, 16);
      wait_vm<0>();
      if (lane == 0) set_flag(flag2 + 128 + e * 8 + (h >> 2));
    }
    STAMP(30);
    return;
  }

  const int w = wave, hh = lane >> 5, r32 = lane & 31;
  unsigned aV[2], aK[4];
  unsigned aMyDone = aDONE + w * 4, aF = aFULL;
  PIN(aMyDone); PIN(aF);
  {
    const unsigned q = i16 >> 2, p = i16 & 3, r8a = 4 * hh + q;
#pragma unroll
    for (int d = 0; d < 2; ++d) aV[d] = ldsb + r8a * 128 + (((4 * d + 2 * (g & 1) + (p >> 1)) ^ mv(r8a)) << 4) + 8 * (p & 1);
  }
  bf16x8 Q[8];
  {
    unsigned xo[4];
#pragma unroll
    for (int t = 0; t < 4; ++t) xo[t] = ldsb + 16384 + w * 4096 + (r32 >> 3) * 1024 + (r32 & 7) * 128 + (((2 * t + hh) ^ mv(r32 & 7) ^ ((r32 >> 3) & 1)) << 4);
    f32x16 acc[4] = {};
    bf16x8 fr[8], bo[2];
    auto rd = [&](auto rc, unsigned so) {
      constexpr int r = decltype(rc)::value;
      const unsigned a = aV[r & 1] + so + (r >> 2) * 4096 + ((r >> 1) & 1) * 1024;
      fr[r & 7] = cat(ldstr(a), ldstr(a + 2048));
    };
    wait2(aF, 1);
    STAMP(1);
    unsigned so = stg_off(0);
    bo[0] = lds128(xo[0] + so);
    rd(IntC<1>{}, so); rd(IntC<0>{}, so); rd(IntC<3>{}, so); rd(IntC<2>{}, so);
    for (int i = 0; i < 8; ++i) {
      unsigned sn = so;
      u32x2 fl = {0, 0};
      static_for<16>([&](auto pc) {
        constexpr int p = decltype(pc)::value;
        acc[p & 3] = mfmaL(fr[p & 7], bo[(p >> 2) & 1], acc[p & 3]); PIN(acc[p & 3]);
        SB();
        if constexpr (p == 12) {
          sn = so + STG; if (sn == STG0 + 3 * STG) sn = STG0;
          if (__builtin_expect((unsigned)__builtin_amdgcn_readfirstlane(min(fl[0], fl[1])) < (unsigned)(i + 2) && i < 7, 0)) {
            PIN(fr[5]); PIN(fr[6]); PIN(fr[7]);
            wait2(aF, i + 2);
          }
          MEMFENCE();
          STAMP(2 + i);
        }
        constexpr int r = ((p + 4) ^ 1) & 15;
        if constexpr ((p & 3) == 0) bo[((p + 4) >> 2) & 1] = lds128(xo[((p + 4) >> 2) & 3] + sn);
        rd(IntC<r>{}, sn);
        if constexpr (p == 8) fl = ldsw2(aF);
        if constexpr (p == 11) { MEMFENCE(); stsw(aMyDone, i + 1); }
        SB();
      });
      so = sn;
    }
    static_for<4>([&](auto dc) {
      constexpr int D = decltype(dc)::value;
      Q[2 * D] = pack8_contig32(quad<0>(acc[D]) * QSCALE, quad<1>(acc[D]) * QSCALE);
      Q[2 * D + 1] = pack8_contig32(quad<2>(acc[D]) * QSCALE, quad<3>(acc[D]) * QSCALE);
    });
  }
#pragma unroll
  for (int t = 0; t < 4; ++t) aK[t] = ldsb + (r32 >> 3) * 2048 + (r32 & 7) * 128 + (((2 * t + hh) ^ mv(r32 & 7) ^ ((r32 >> 3) & 1)) << 4);
  aV[0] += SLOT; aV[1] += SLOT;

  f32x16 O[4] = {}, nm = {}, Sa, Sb;
  float l0 = 0.f, l1 = 0.f;
  auto max16 = [&](const f32x16& S) {
    float a = max3f(S[0], S[1], S[2]), c = max3f(S[3], S[4], S[5]);
    a = max3f(a, S[6], S[7]); c = max3f(c, S[8], S[9]); a = max3f(a, S[10], S[11]); c = max3f(c, S[12], S[13]);
    return max2f(max3f(a, S[14], S[15]), c);
  };
  auto fixL = [&](f32x16& S, bool first) {
    float d = xmax32(max16(S));
    if (!first) d = fmaxf(d, 0.f);
    S -= d; nm -= d;
    if (!first) {
      const float al = ex2(-d);
#pragma unroll
      for (int D = 0; D < 4; ++D) O[D] *= al;
      l0 *= al; l1 *= al;
    }
  };

  bf16x8 fr[8];
  wait2(aF, 9);
  STAMP(10);
#pragma unroll
  for (int s = 0; s < 8; ++s) Sa = mfmaL(lds128(aK[s & 3] + (s >> 2) * 1024), Q[s], s ? Sa : nm);
  fr[1] = lds128(aK[1] + 8192); fr[0] = lds128(aK[0] + 8192); fr[3] = lds128(aK[3] + 8192); fr[2] = lds128(aK[2] + 8192);
  fixL(Sa, true);
  SB();

  int sk = 0, sv = 1;
  unsigned step = 10;
  auto body = [&](auto lastc) {
    constexpr bool LAST = decltype(lastc)::value;
    unsigned P[8];
    int mx = 0, my = 0;
    u32x2 fl = {0, 0};
    auto rd = [&](auto rc) {
      constexpr int r = decltype(rc)::value, rj = r & 7;
      if constexpr (r < 8) fr[rj] = lds128(aK[rj & 3] + 8192 + (rj >> 2) * 1024);
      else if constexpr (r < 16 || (r >= 24 && r < 32)) {
        const unsigned a = aV[rj & 1] + (r >= 24 ? 8192 : 0) + (rj >> 2) * 4096 + ((rj >> 1) & 1) * 1024;
        fr[rj] = cat(ldstr(a), ldstr(a + 2048));
      } else if constexpr (!LAST) fr[rj] = lds128(aK[rj & 3] + (r >= 32 ? 8192 : 0) + (rj >> 2) * 1024);
    };
    static_for<32>([&](auto pc) {
      constexpr int p = decltype(pc)::value, j = p & 7;
      if constexpr (p == 4 || (p == 12 && !LAST)) {
        if (__builtin_expect((unsigned)__builtin_amdgcn_readfirstlane(min(fl[0], fl[1])) < step, 0)) {
          PIN(fr[(p + 0) & 7]); PIN(fr[(p + 1) & 7]); PIN(fr[(p + 2) & 7]); PIN(fr[(p + 3) & 7]);
          wait2(aF, step);
        }
        MEMFENCE();
        if constexpr (p == 4) stsw(aMyDone, step - 1);
        ++step;
        if constexpr (p == 12) {
          const int dk = sk >= NSLOT - 2 ? (2 - NSLOT) * SLOT : 2 * SLOT;
          sk += sk >= NSLOT - 2 ? 2 - NSLOT : 2;
#pragma unroll
          for (int t = 0; t < 4; ++t) aK[t] += dk;
        }
      }
      if constexpr ((p & 1) == 0) {
        if constexpr (p == 0 || (p == 8 && !LAST)) fl = ldsw2(aF);
        rd(IntC<p + 5>{}); rd(IntC<p + 4>{});
      }
      if constexpr (p < 8 || (p >= 16 && p < 24)) {
        constexpr bool A = p < 8;
        f32x16& Sn = A ? Sb : Sa; f32x16& So = A ? Sa : Sb;
        if constexpr (A || !LAST) { Sn = mfmaL(fr[j], Q[j], j ? Sn : nm); PIN(Sn); }
        if constexpr ((j & 1) == 0) { So[2 * j] = ex2(So[2 * j]); }
        else {
          So[2 * j - 1] = ex2(So[2 * j - 1]); So[2 * j] = ex2(So[2 * j]); So[2 * j + 1] = ex2(So[2 * j + 1]);
          if constexpr (j > 1) { P[j - 3] = cvtpk(So[2 * j - 6], So[2 * j - 5]); P[j - 2] = cvtpk(So[2 * j - 4], So[2 * j - 3]); PIN(P[j - 3]); PIN(P[j - 2]); }
        }
        PIN(So);
      } else {
        constexpr bool A = p < 16;
        f32x16& Sn = A ? Sb : Sa; f32x16& So = A ? Sa : Sb;
        constexpr int t = j >> 2, D = j & 3;
        const u32x4 pw = {P[4 * t], P[4 * t + 1], P[4 * t + 2], P[4 * t + 3]};
        O[D] = mfmaL(fr[j], __builtin_bit_cast(bf16x8, pw), O[D]); PIN(O[D]);
        if constexpr (j == 0) { P[6] = cvtpk(So[12], So[13]); P[7] = cvtpk(So[14], So[15]); PIN(P[6]); PIN(P[7]); }
        if constexpr (j & 1) {
          l0 += So[2 * j - 2]; PIN(l0); l1 += So[2 * j - 1]; PIN(l1); l0 += So[2 * j]; PIN(l0); l1 += So[2 * j + 1]; PIN(l1);
          if constexpr (A || !LAST) {
            if constexpr (j == 1) { mx = imax3(Sn[0], Sn[1], Sn[2]); my = imax3(Sn[3], Sn[4], Sn[5]); }
            if constexpr (j == 3) { mx = imax3(mx, Sn[6], Sn[7]); my = imax3(my, Sn[8], Sn[9]); }
            if constexpr (j == 5) { mx = imax3(mx, Sn[10], Sn[11]); my = imax3(my, Sn[12], Sn[13]); }
            if constexpr (j == 7) { mx = imax3(mx, Sn[14], Sn[15]); mx = max(mx, my); }
            PIN(mx); PIN(my);
          }
        }
        if constexpr (j == 7 && (A || !LAST)) {
          SB();
          if (__builtin_expect(__any(mx > __float_as_int(THR)), 0)) {
            if constexpr (A && !LAST) { PIN(fr[0]); PIN(fr[1]); PIN(fr[2]); PIN(fr[3]); }
            fixL(Sn, false);
          }
        }
        if constexpr (p == 27) {
          const int dv = sv >= NSLOT - 2 ? (2 - NSLOT) * SLOT : 2 * SLOT;
          sv += sv >= NSLOT - 2 ? 2 - NSLOT : 2;
          aV[0] += dv; aV[1] += dv;
          if constexpr (!LAST) { MEMFENCE(); stsw(aMyDone, step - 2); }
        }
      }
      SB();
    });
  };
  for (int n = 0; n < 15; ++n) { STAMP(11 + n); body(BoolC<false>{}); }
  STAMP(26);
  body(BoolC<true>{});
  STAMP(27);

  int ln = __builtin_amdgcn_mbcnt_hi(~0u, __builtin_amdgcn_mbcnt_lo(~0u, 0u));
  PIN(ln);
  const unsigned l16 = ln * 16;
  bf16x8 fo[32];
  {
    const float rl = __builtin_amdgcn_rcpf(xadd32(l0 + l1));
    static_for<4>([&](auto dc) {
      constexpr int D = decltype(dc)::value;
      fo[2 * D] = pack8(quad<0>(O[D]) * rl, quad<1>(O[D]) * rl);
      fo[2 * D + 1] = pack8(quad<2>(O[D]) * rl, quad<3>(O[D]) * rl);
      __builtin_amdgcn_raw_buffer_store_b128(__builtin_bit_cast(u32x4, fo[2 * D]), rof, l16, ((h & 3) * 36 + w * 8 + 2 * D) * 1024, 16);
      __builtin_amdgcn_raw_buffer_store_b128(__builtin_bit_cast(u32x4, fo[2 * D + 1]), rof, l16, ((h & 3) * 36 + w * 8 + 2 * D + 1) * 1024, 16);
    });
  }
  {
    const unsigned g2 = ln >> 4, q = (ln & 15) >> 2, p = ln & 3, r8a = 4 * (ln >> 5) + q;
    const unsigned v0 = ldsb + r8a * 128 + (((2 * (g2 & 1) + (p >> 1)) ^ mv(r8a)) << 4) + 8 * (p & 1);
    const unsigned v1 = ldsb + r8a * 128 + (((4 + 2 * (g2 & 1) + (p >> 1)) ^ mv(r8a)) << 4) + 8 * (p & 1);
    f32x16 acc[4] = {};
    GAS unsigned* fl2 = flag2 + w * 32 + (h >> 2);
    unsigned av0 = 0, av1 = 0;
    auto rd = [&](auto rc) {
      constexpr int r = decltype(rc)::value;
      if constexpr (r < 128) {
        constexpr int i = r < 32 ? r >> 4 : 2 + ((r - 32) % 24) / 4;
        constexpr int t = r < 32 ? (r >> 2) & 3 : r & 3, D = r < 32 ? r & 3 : (r - 32) / 24;
        const unsigned a = ((D & 1) ? av1 : av0) + t * 4096 + (D >> 1) * 1024;
        fr[r & 7] = cat(ldstr(a), ldstr(a + 2048));
        (void)i;
      }
    };
    wait2(aWOF, 2);
    static_for<132>([&](auto rc) {
      constexpr int r = decltype(rc)::value, pp = r - 4;
      if constexpr (pp == 8) {
        wait_vm<0>();
        if (ln == 0) set_flag(flag1 + (h & 3) * 32 + w);
        STAMP(28);
      }
      if constexpr (pp == 24) {
        wait_flags(flag1 + (ln & 3) * 32 + w);
        __builtin_amdgcn_fence(__ATOMIC_ACQUIRE, "wavefront");
        STAMP(29);
#pragma unroll
        for (int k = 8; k < 32; ++k)
          fo[k] = __builtin_bit_cast(bf16x8, __builtin_amdgcn_raw_buffer_load_b128(rof, l16, (((h + (k >> 3)) & 3) * 36 + w * 8 + (k & 7)) * 1024, 16));
      }
      if constexpr (pp >= 0) {
        constexpr int k = pp < 32 ? pp >> 2 : 8 + (pp - 32) % 24, D = pp < 32 ? pp & 3 : (pp - 32) / 24;
        acc[D] = mfmaL(fr[pp & 7], fo[k], acc[D]); PIN(acc[D]);
        SB();
      }
      if constexpr (r == 32) { wait2(aWOF, 8); STAMP(30); }
      if constexpr (r < 128) {
        if constexpr ((r < 32 && (r & 15) == 0) || (r >= 32 && (r & 3) == 0)) {
          constexpr int i = r < 32 ? r >> 4 : 2 + ((r - 32) % 24) / 4;
          unsigned so = wo_slot(i);
          asm volatile("" : "+s"(so));
          av0 = v0 + so; av1 = v1 + so;
        }
        rd(IntC<(r ^ 1)>{});
      }
      if constexpr (pp >= 0) {
        constexpr int D = pp < 32 ? pp & 3 : (pp - 32) / 24;
        if constexpr (pp >= 32 && (pp - 32) % 24 == 23) {
          SB();
          if constexpr (D > 0) {
            wait_vm<0>();
            if (ln == 0) set_flag(fl2 + (D - 1) * 8);
          }
          __builtin_amdgcn_raw_buffer_store_b128(__builtin_bit_cast(u32x4, pack8(quad<0>(acc[D]), quad<1>(acc[D]))), rp, l16, (w * 8 + D * 2) * 1024, 16);
          __builtin_amdgcn_raw_buffer_store_b128(__builtin_bit_cast(u32x4, pack8(quad<2>(acc[D]), quad<3>(acc[D]))), rp, l16, (w * 8 + D * 2 + 1) * 1024, 16);
          STAMP(31 + D);
        }
      }
      SB();
    });
    wait_vm<0>();
    if (ln == 0) set_flag(fl2 + 3 * 8);
  }
  STAMP(35);
}

extern "C" void kernel_launch(void* const* d_in, const int* in_sizes, int n_in, void* d_out, int out_size, void* d_ws,
                              size_t ws_size, hipStream_t stream) {
  const uint16_t* x = (const uint16_t*)d_in[0];
  const uint16_t* kc = (const uint16_t*)d_in[1];
  const uint16_t* vc = (const uint16_t*)d_in[2];
  const uint16_t* wq = (const uint16_t*)d_in[3];
  const uint16_t* wo = (const uint16_t*)d_in[4];
  (void)hipFuncSetAttribute((const void*)attn_kernel, hipFuncAttributeMaxDynamicSharedMemorySize, LDS_BYTES);
  (void)hipMemsetAsync(d_ws, 0, WS_FLAG_BYTES, stream);
  hipLaunchKernelGGL(attn_kernel, dim3(256), dim3(512), LDS_BYTES, stream, x, kc, vc, wq, wo, (char*)d_ws, (uint16_t*)d_out);
}
```

```cpp
#include <hip/hip_runtime.h>
#include <hip/hip_bf16.h>
#include <stdint.h>
#include <utility>

typedef __bf16 bf16x8 __attribute__((ext_vector_type(8)));
typedef __bf16 bf16x4 __attribute__((ext_vector_type(4)));
typedef __bf16 bf16x2 __attribute__((ext_vector_type(2)));
typedef short s16x4 __attribute__((ext_vector_type(4)));
typedef float f32x16 __attribute__((ext_vector_type(16)));
typedef float f32x4 __attribute__((ext_vector_type(4)));
typedef float f32x2 __attribute__((ext_vector_type(2)));
typedef unsigned u32x4 __attribute__((ext_vector_type(4)));
typedef unsigned u32x2 __attribute__((ext_vector_type(2)));
#define LAS __attribute__((address_space(3)))
#define GAS __attribute__((address_space(1)))
#define DI __device__ __forceinline__

constexpr int NB = 8, SQ = 144, DM = 512, NH = 32, SK = 1024, DH = 128;
constexpr int SLOT = 16384, NSLOT = 6, LDS_BYTES = 163840;
constexpr int STG0 = 3 * SLOT, STG = 34 * 1024, MISC_OFF = STG0 + 3 * STG;
constexpr int XCH_OFF = MISC_OFF;
constexpr int ML_OFF = MISC_OFF + 8192;
constexpr int CTL_OFF = MISC_OFF + 9216;
DI int stg_off(int i) { return STG0 + ((i + 1) % 3) * STG; }
#define KV_AUX 2
#define EARLY_MASK 0x54
#define WO_BYTES (NH * DH * DM * 2)
#define WO_AUX 2
#define WX_AUX 0
constexpr size_t WS_FLAG_BYTES = 65536 + 8192, WS_OF = WS_FLAG_BYTES, WS_PART = WS_OF + (size_t)256 * 36 * 1024, WS_END = WS_PART + (size_t)256 * 36 * 1024;
constexpr int SPIN_CAP = 1 << 16;
constexpr float QSCALE = 0.088388347648318440f * 1.4426950408889634f;
constexpr float THR = 8.0f;

DI f32x16 mfmaL(bf16x8 a, bf16x8 b, f32x16 c) { return __builtin_amdgcn_mfma_f32_32x32x16_bf16(a, b, c, 0, 0, 0); }
DI f32x4 mfma32(bf16x8 a, bf16x8 b, f32x4 c) { return __builtin_amdgcn_mfma_f32_16x16x32_bf16(a, b, c, 0, 0, 0); }
DI f32x4 mfma16(bf16x4 a, bf16x4 b, f32x4 c) {
  return __builtin_amdgcn_mfma_f32_16x16x16bf16_1k(__builtin_bit_cast(s16x4, a), __builtin_bit_cast(s16x4, b), c, 0, 0, 0);
}
DI unsigned cvtpk(float lo, float hi) { f32x2 v = {lo, hi}; bf16x2 b = __builtin_convertvector(v, bf16x2); return __builtin_bit_cast(unsigned, b); }
DI float max3f(float a, float b, float c) { return fmaxf(fmaxf(a, b), c); }
DI float max2f(float a, float b) { return fmaxf(a, b); }
DI int imax3(float a, float b, float c) { return max(max(__float_as_int(a), __float_as_int(b)), __float_as_int(c)); }
DI int imax3(int a, float b, float c) { return max(max(a, __float_as_int(b)), __float_as_int(c)); }
DI float ex2(float x) { return __builtin_amdgcn_exp2f(x); }
DI bf16x8 lds128(unsigned addr) { return __builtin_bit_cast(bf16x8, *(const volatile LAS u32x4*)(uintptr_t)addr); }
DI bf16x4 ldstr(unsigned addr) { return __builtin_amdgcn_ds_read_tr16_b64_v4bf16((LAS bf16x4*)(uintptr_t)addr); }
DI bf16x8 cat(bf16x4 a, bf16x4 b) { return __builtin_shufflevector(a, b, 0, 1, 2, 3, 4, 5, 6, 7); }
#define BAR() asm volatile("s_waitcnt lgkmcnt(0)\n\ts_barrier" ::: "memory")
#define RAWBAR() asm volatile("s_barrier" ::: "memory")
#define SB() __builtin_amdgcn_sched_barrier(0)
#define PIN(x) asm volatile("" : "+v"(x))
template <int I> struct IntC { static constexpr int value = I; };
template <bool V> struct BoolC { static constexpr bool value = V; };
template <class F, int... Is> DI void static_for_(F&& f, std::integer_sequence<int, Is...>) { (f(IntC<Is>{}), ...); }
template <int N, class F> DI void static_for(F&& f) { static_for_(f, std::make_integer_sequence<int, N>{}); }
DI float xmax32(float x) {
  auto b = __builtin_amdgcn_permlane32_swap(__float_as_uint(x), __float_as_uint(x), false, false);
  return max2f(__uint_as_float(b[0]), __uint_as_float(b[1]));
}
DI float xmax16(float x) {
  auto a = __builtin_amdgcn_permlane16_swap(__float_as_uint(x), __float_as_uint(x), false, false);
  return max2f(__uint_as_float(a[0]), __uint_as_float(a[1]));
}
DI float xadd32(float x) {
  auto b = __builtin_amdgcn_permlane32_swap(__float_as_uint(x), __float_as_uint(x), false, false);
  return __uint_as_float(b[0]) + __uint_as_float(b[1]);
}
DI float xadd16(float x) {
  auto b = __builtin_amdgcn_permlane16_swap(__float_as_uint(x), __float_as_uint(x), false, false);
  return __uint_as_float(b[0]) + __uint_as_float(b[1]);
}
DI bf16x8 pack8(f32x4 a, f32x4 b) {
  u32x4 w = {cvtpk(a[0], a[1]), cvtpk(a[2], a[3]), cvtpk(b[0], b[1]), cvtpk(b[2], b[3])};
  return __builtin_bit_cast(bf16x8, w);
}
template <int Q> DI f32x4 quad(const f32x16& v) { return f32x4{v[4 * Q], v[4 * Q + 1], v[4 * Q + 2], v[4 * Q + 3]}; }
DI bf16x8 pack8_contig16(f32x4 x, f32x4 y) {
  unsigned x0 = cvtpk(x[0], x[1]), x1 = cvtpk(x[2], x[3]), y0 = cvtpk(y[0], y[1]), y1 = cvtpk(y[2], y[3]);
  auto a0 = __builtin_amdgcn_permlane32_swap(x0, y0, false, false);
  auto a1 = __builtin_amdgcn_permlane32_swap(x1, y1, false, false);
  auto b0 = __builtin_amdgcn_permlane16_swap(a0[0], a0[1], false, false);
  auto b1 = __builtin_amdgcn_permlane16_swap(a1[0], a1[1], false, false);
  u32x4 w = {b0[0], b1[0], b0[1], b1[1]};
  return __builtin_bit_cast(bf16x8, w);
}
DI bf16x8 pack8_contig32(f32x4 x, f32x4 y) {
  unsigned x0 = cvtpk(x[0], x[1]), x1 = cvtpk(x[2], x[3]), y0 = cvtpk(y[0], y[1]), y1 = cvtpk(y[2], y[3]);
  auto a0 = __builtin_amdgcn_permlane32_swap(x0, y0, false, false);
  auto a1 = __builtin_amdgcn_permlane32_swap(x1, y1, false, false);
  u32x4 w = {a0[0], a1[0], a0[1], a1[1]};
  return __builtin_bit_cast(bf16x8, w);
}
template <int N> DI void wait_vm() { asm volatile("s_waitcnt vmcnt(%0)" ::"i"(N) : "memory"); }
DI void wait_flags(GAS unsigned* fp) {
#pragma unroll 1
  for (int it = 0; it < SPIN_CAP; ++it) {
    if (__all(__hip_atomic_load(fp, __ATOMIC_RELAXED, __HIP_MEMORY_SCOPE_AGENT) == 1u)) break;
    __builtin_amdgcn_s_sleep(1);
  }
}
DI __amdgpu_buffer_rsrc_t mk_rsrc(const void* p, unsigned bytes) { return __builtin_amdgcn_make_buffer_rsrc((void*)p, 0, bytes, 0x00020000); }
template <int AUX> DI void dma16(__amdgpu_buffer_rsrc_t r, unsigned ldsaddr, unsigned voff, unsigned soff) {
  __builtin_amdgcn_raw_ptr_buffer_load_lds(r, (LAS void*)(uintptr_t)ldsaddr, 16, voff, soff, 0, AUX);
}
DI unsigned mv(unsigned r8) { return ((r8 & 2) << 1) | ((r8 & 4) >> 1); }

template <class T> DI T sel(bool c, T a, T b) { return c ? a : b; }
DI unsigned ldsw(unsigned addr) { return *(const volatile LAS unsigned*)(uintptr_t)addr; }
DI u32x2 ldsw2(unsigned addr) { return *(const volatile LAS u32x2*)(uintptr_t)addr; }
DI u32x4 ldsw4(unsigned addr) { return *(const volatile LAS u32x4*)(uintptr_t)addr; }
DI void stsw(unsigned addr, unsigned v) { *(volatile LAS unsigned*)(uintptr_t)addr = v; }
#define MEMFENCE() asm volatile("" ::: "memory")
DI void pub(unsigned addr, unsigned v) {
  asm volatile("s_mov_b64 exec, 1\n\tds_max_u32 %0, %1\n\ts_mov_b64 exec, -1" ::"v"(addr), "v"(v) : "memory");
}
DI void wait2(unsigned addr, unsigned need) {
#pragma unroll 1
  for (int it = 0; it < SPIN_CAP; ++it) {
    const u32x2 v = ldsw2(addr);
    if ((unsigned)__builtin_amdgcn_readfirstlane(min(v[0], v[1])) >= need) break;
    __builtin_amdgcn_s_sleep(1);
  }
  MEMFENCE();
}
DI void wait1(unsigned addr, unsigned need) {
#pragma unroll 1
  for (int it = 0; it < SPIN_CAP; ++it) {
    if ((unsigned)__builtin_amdgcn_readfirstlane(ldsw(addr)) >= need) break;
    __builtin_amdgcn_s_sleep(1);
  }
  MEMFENCE();
}
DI unsigned done_min(unsigned addr) {
  u32x4 a; u32x2 c;
  asm volatile("ds_read_b128 %0, %2\n\tds_read_b64 %1, %2 offset:16\n\ts_waitcnt lgkmcnt(0)" : "=&v"(a), "=&v"(c) : "v"(addr) : "memory");
  return __builtin_amdgcn_readfirstlane(min(min(min(a[0], a[1]), min(a[2], a[3])), min(c[0], c[1])));
}
DI void set_flag(GAS unsigned* p) { __hip_atomic_store(p, 1u, __ATOMIC_RELAXED, __HIP_MEMORY_SCOPE_AGENT); }
DI unsigned wo_slot(int i) { return (i < 3 ? 6 + i : (32 + i - 3) % NSLOT) * SLOT; }

#define STAMP(idx)
#define LSTAMP(idx)

DI f32x4 bf_lo(u32x4 v) { return f32x4{__uint_as_float(v[0] << 16), __uint_as_float(v[0] & 0xffff0000u), __uint_as_float(v[1] << 16), __uint_as_float(v[1] & 0xffff0000u)}; }
DI f32x4 bf_hi(u32x4 v) { return f32x4{__uint_as_float(v[2] << 16), __uint_as_float(v[2] & 0xffff0000u), __uint_as_float(v[3] << 16), __uint_as_float(v[3] & 0xffff0000u)}; }
DI void reduce_blocks(char* ws, uint16_t* out, int b, int h, int L, int lane) {
  const int hg = h >> 2, nt = h & 3;
  const __amdgpu_buffer_rsrc_t rp = mk_rsrc(ws + WS_PART + (size_t)(b * 4 + nt) * 8 * 36 * 1024, 8 * 36 * 1024);
  GAS unsigned* flag2 = (GAS unsigned*)(ws + 32768) + (b * 4 + nt) * 256;
  __builtin_amdgcn_s_setprio(0);
  auto sum8 = [&](const u32x4(&v)[8], f32x4& x, f32x4& y) {
    x = ((bf_lo(v[0]) + bf_lo(v[1])) + (bf_lo(v[2]) + bf_lo(v[3]))) + ((bf_lo(v[4]) + bf_lo(v[5])) + (bf_lo(v[6]) + bf_lo(v[7])));
    y = ((bf_hi(v[0]) + bf_hi(v[1])) + (bf_hi(v[2]) + bf_hi(v[3]))) + ((bf_hi(v[4]) + bf_hi(v[5])) + (bf_hi(v[6]) + bf_hi(v[7])));
  };
  {
    wait_flags(flag2 + ((2 * L + ((lane >> 3) & 1)) * 4 + (hg >> 1)) * 8 + (lane & 7));
    __builtin_amdgcn_fence(__ATOMIC_ACQUIRE, "wavefront");
    u32x4 v0[8], v1[8];
#pragma unroll
    for (int u = 0; u < 8; ++u) {
      v0[u] = __builtin_amdgcn_raw_buffer_load_b128(rp, lane * 16, (u * 36 + 16 * L + hg) * 1024, 16);
      v1[u] = __builtin_amdgcn_raw_buffer_load_b128(rp, lane * 16, (u * 36 + 16 * L + 8 + hg) * 1024, 16);
    }
    f32x4 x, y;
    uint16_t* o = out + ((size_t)(b * SQ + L * 64 + (lane & 31)) * DM + nt * 128 + hg * 16 + (lane >> 5) * 8);
    sum8(v0, x, y); *(u32x4*)o = __builtin_bit_cast(u32x4, pack8_contig32(x, y));
    sum8(v1, x, y); *(u32x4*)(o + 32 * DM) = __builtin_bit_cast(u32x4, pack8_contig32(x, y));
  }
  if (hg < 2) {
    const int wp = 2 * hg + L;
    wait_flags(flag2 + 128 + (wp >> 1) * 8 + (lane & 7));
    __builtin_amdgcn_fence(__ATOMIC_ACQUIRE, "wavefront");
    u32x4 v[8];
#pragma unroll
    for (int u = 0; u < 8; ++u) v[u] = __builtin_amdgcn_raw_buffer_load_b128(rp, lane * 16, (u * 36 + 32 + wp) * 1024, 16);
    f32x4 x, y; sum8(v, x, y);
    *(u32x4*)(out + ((size_t)(b * SQ + 128 + (lane & 15)) * DM + nt * 128 + wp * 32 + (lane >> 4) * 8)) = __builtin_bit_cast(u32x4, pack8_contig16(x, y));
  }
}

struct Handshake {
  GAS unsigned* base;
  GAS unsigned* mine;
  unsigned v, lastL, N; int phase; bool done, first, leader; int h;
  DI void init(char* ws, int b, int h_, int lane, GAS unsigned* flag1, GAS unsigned* flag2) {
    h = h_; base = (GAS unsigned*)(ws + 65536) + b * 256;
    GAS unsigned* c = lane < 6 ? flag1 + (h & 3) * 32 + lane
                    : lane < 22 ? flag2 + (lane - 6) * 8 + (h >> 2)
                    : lane < 24 ? flag2 + 128 + (lane - 22) * 8 + (h >> 2) : base + 64 + h;
    if (lane < 25) __hip_atomic_store(c, 0u, __ATOMIC_RELAXED, __HIP_MEMORY_SCOPE_AGENT);
    wait_vm<0>();
    mine = lane < 32 ? base + lane : (lane == 32 ? base + 32 : base + 64 + h);
    leader = h == 0; phase = 0; done = false; first = true; lastL = 0; N = 0;
    v = __hip_atomic_load(mine, __ATOMIC_RELAXED, __HIP_MEMORY_SCOPE_AGENT);
  }
  DI void step(int lane, unsigned aHS) {
    if (done) return;
    const unsigned vL = __builtin_amdgcn_readlane(v, 32), vI = __builtin_amdgcn_readlane(v, 33);
    if (!first && vI == 1u) { done = true; stsw(aHS, 1); return; }
    if (leader) {
      if (phase == 0) {
        N = vL + 1;
        for (int t = 0; t < 34 && __any(lane < 32 && v == N); ++t) { ++N; if (N == vL) ++N; }
        if (lane == 0) __hip_atomic_store(base + 32, N, __ATOMIC_RELAXED, __HIP_MEMORY_SCOPE_AGENT);
        phase = 1;
      } else if (phase == 1 && __all(lane >= 32 || v == N)) {
        if (lane < 32) __hip_atomic_store(base + 64 + lane, 1u, __ATOMIC_RELAXED, __HIP_MEMORY_SCOPE_AGENT);
        phase = 2;
      }
    }
    if (first || vL != lastL) {
      if (lane == 0) __hip_atomic_store(base + h, vL, __ATOMIC_RELAXED, __HIP_MEMORY_SCOPE_AGENT);
      lastL = vL; first = false;
    }
    v = __hip_atomic_load(mine, __ATOMIC_RELAXED, __HIP_MEMORY_SCOPE_AGENT);
  }
};

__global__ __launch_bounds__(512, 2) void attn_kernel(const uint16_t* __restrict__ x, const uint16_t* __restrict__ kc,
                                                      const uint16_t* __restrict__ vc, const uint16_t* __restrict__ wq,
                                                      const uint16_t* __restrict__ wo, char* __restrict__ ws,
                                                      uint16_t* __restrict__ out) {
  extern __shared__ __attribute__((aligned(16))) char lds[];
  const int tid = threadIdx.x, lane = tid & 63;
  const int wave = __builtin_amdgcn_readfirstlane(tid >> 6);
  const int bid = blockIdx.x;
  const int h = 4 * (bid & 7) + ((bid >> 3) & 3), b = bid >> 5;
  const unsigned ldsb = (unsigned)(uintptr_t)(LAS char*)lds;
  const unsigned aFULL = ldsb + CTL_OFF, aWOF = aFULL + 8, aDONE = aFULL + 16, aPAIR = aFULL + 48, aHS = aFULL + 56;
  const __amdgpu_buffer_rsrc_t rof = mk_rsrc(ws + WS_OF + (size_t)(b * NH + (h & ~3)) * 36 * 1024, 4 * 36 * 1024);
  GAS unsigned* flag1 = (GAS unsigned*)ws + (b * NH + (h & ~3)) * 32;
  GAS unsigned* flag2 = (GAS unsigned*)(ws + 32768) + (b * 4 + (h & 3)) * 256;
  const __amdgpu_buffer_rsrc_t rp = mk_rsrc(ws + WS_PART + (size_t)((b * 4 + (h & 3)) * 8 + (h >> 2)) * 36 * 1024, 36 * 1024);

  if (wave >= 6) {
    __builtin_amdgcn_s_setprio(3);
    const unsigned half = wave - 6, r8 = lane >> 3, sw = ((lane & 7) ^ mv(r8)) << 4;
    const unsigned aMy = aFULL + half * 4, aMyW = aWOF + half * 4;
    const size_t hb = (size_t)(b * NH + h) * SK * DH;
    const __amdgpu_buffer_rsrc_t rk = mk_rsrc(kc + hb, SK * DH * 2), rv = mk_rsrc(vc + hb, SK * DH * 2), rw = mk_rsrc(wo, WO_BYTES);
    const unsigned voff = r8 * 256u + sw, voffx = voff ^ 16u, voffw = r8 * 1024u + sw;
    const unsigned aPoll = aDONE + (lane < 6 ? lane : 0) * 4;
    auto busy = [&](unsigned need) {
      unsigned v;
      asm volatile("ds_read_b32 %0, %1\n\ts_waitcnt lgkmcnt(0)" : "=v"(v) : "v"(aPoll) : "memory");
      return __any(v < need);
    };
    const unsigned dsth = ldsb + half * 8192;
    unsigned src = half * 8192u;
    auto dmaK = [&](unsigned dst) {
#pragma unroll
      for (int m = 0; m < 8; ++m) dma16<KV_AUX>(rk, dst + m * 1024, (m & 2) ? voffx : voff, src + (m >> 1) * 2048 + (m & 1) * 128);
    };
    auto dmaV = [&](unsigned dst) {
#pragma unroll
      for (int m = 0; m < 8; ++m) dma16<KV_AUX>(rv, dst + m * 1024, voff, src + (m >> 1) * 2048 + (m & 1) * 128);
      src += 16384u;
    };
    auto dmaW = [&](unsigned dst, int i) {
      const unsigned sw_ = (unsigned)((h >> 2) * 512 + ((h + (i >> 1)) & 3) * 128 + (i & 1) * 64 + half * 32) * 1024u + (h & 3) * 256u;
#pragma unroll
      for (int m = 0; m < 8; ++m) dma16<WO_AUX>(rw, dst + m * 1024, voffw, sw_ + (m >> 1) * 8192 + (m & 1) * 128);
    };
    {
      const bool isw = half == 0;
      const __amdgpu_buffer_rsrc_t rs = isw ? mk_rsrc(wq, DM * NH * DH * 2) : mk_rsrc(x + (size_t)b * SQ * DM, SQ * DM * 2);
      const unsigned voff0 = isw ? ((r8 & 3) + 8 * (r8 >> 2)) * 8192u + sw : r8 * 1024u + sw;
      const unsigned voff1 = isw ? voff0 + 128u : (voff0 ^ 16u) + 8192u;
      const int rot = b + 2 * (h & 3);
      const unsigned pstep0 = isw ? 4u * 8192u : 16384u, pstep1 = isw ? 12u * 8192u : 16384u;
      auto issue = [&](int st) {
        unsigned dst = ldsb + stg_off(st) + (isw ? 0 : 16384);
        const int i = (st + rot) & 7;
        unsigned so = isw ? (unsigned)i * 64u * 8192u + (unsigned)h * 256u : (unsigned)i * 128u;
#pragma unroll
        for (int m = 0; m < 8; ++m) {
          dma16<WX_AUX>(rs, dst, voff0, so); dma16<WX_AUX>(rs, dst + 1024, voff1, so);
          dst += 2048; so += (m & 1) ? pstep1 : pstep0;
        }
        if (!isw) { dma16<WX_AUX>(rs, dst, voff0, so); dma16<WX_AUX>(rs, dst + 1024, voff1, so); }
      };
      issue(0);
      BAR();
      bool kvb = false;
      int je = 0;
#pragma unroll 1
      for (int st = 1; st < 8; ++st) {
        if (st >= 3 && busy(st - 2)) {
          if (kvb) wait_vm<8>(); else wait_vm<0>();
          pub(aMy, st);
#pragma unroll 1
          for (int it = 0; it < SPIN_CAP && busy(st - 2); ++it) __builtin_amdgcn_s_sleep(1);
        }
        issue(st);
        if (kvb) { if (isw) wait_vm<24>(); else wait_vm<26>(); }
        else { if (isw) wait_vm<16>(); else wait_vm<18>(); }
        pub(aMy, st);
        kvb = (EARLY_MASK >> st) & 1;
        if (kvb) { if (je == 1) dmaV(dsth + SLOT); else dmaK(dsth + je * SLOT); ++je; }
      }
    }
    unsigned full = 11;
    auto acquire = [&](unsigned need) {
      if (busy(need)) {
        wait_vm<16>(); pub(aMy, full - 2);
        if (busy(need)) {
          wait_vm<8>(); pub(aMy, full - 1);
          if (busy(need)) {
            wait_vm<0>(); pub(aMy, full);
#pragma unroll 1
            for (int it = 0; it < SPIN_CAP && busy(need); ++it) __builtin_amdgcn_s_sleep(1);
          }
        }
      }
    };
    if (busy(6)) {
      wait_vm<0>(); pub(aMy, 11);
#pragma unroll 1
      for (int it = 0; it < SPIN_CAP && busy(6); ++it) __builtin_amdgcn_s_sleep(1);
    }
    dmaV(dsth + 3 * SLOT); wait_vm<8>(); pub(aMy, 11); full = 12;
    dmaK(dsth + 4 * SLOT); full = 13;
    acquire(7); dmaV(dsth + 5 * SLOT); full = 14;
#pragma unroll 1
    for (int it6 = 0; it6 < 5; ++it6) {
      static_for<6>([&](auto sc) {
        constexpr int sl = decltype(sc)::value;
        if (full < 40) {
          acquire(full - 5);
          if constexpr (sl & 1) dmaV(dsth + sl * SLOT); else dmaK(dsth + sl * SLOT);
          ++full;
          wait_vm<24>(); pub(aMy, full - 3);
        } else {
          acquire(35 + (sl - 2));
          dmaW(dsth + sl * SLOT, 3 + (sl - 2));
          wait_vm<24>(); pub(aMy, sl < 4 ? 36 + sl : 40);
        }
      });
      if (it6 < 3) dmaW(dsth + (6 + it6) * SLOT, it6);
      if (it6 == 3) pub(aMyW, 3);
    }
    wait_vm<0>(); pub(aMy, 40); pub(aMyW, 7);
#pragma unroll 1
    for (int it = 0; it < SPIN_CAP && busy(39); ++it) __builtin_amdgcn_s_sleep(1);
    dmaW(dsth + 0 * SLOT, 7);
    wait_vm<0>(); pub(aMyW, 8);
    wait1(aHS, 1);
    reduce_blocks(ws, out, b, h, wave - 6, lane);
    STAMP(35);
    return;
  }

  if (wave == 0 && lane < 16) stsw(aFULL + lane * 4, 0);
  BAR();
  STAMP(0);
  const int g = lane >> 4, i16 = lane & 15;

  if (wave >= 4) {
    const int e = wave - 4;
    const unsigned aMyDone = aDONE + wave * 4;
    const unsigned q = i16 >> 2, p = i16 & 3, r8b = 4 * (g & 1) + q;
    unsigned aT[4];
#pragma unroll
    for (int t = 0; t < 4; ++t) aT[t] = ldsb + r8b * 128 + (((2 * t + (p >> 1)) ^ mv(r8b)) << 4) + 8 * (p & 1);
    unsigned aR[2];
#pragma unroll
    for (int t = 0; t < 2; ++t) aR[t] = (i16 & 7) * 128 + (((4 * t + g) ^ mv(i16 & 7) ^ (i16 >> 3)) << 4);
    Handshake hs;
    hs.done = true;
    if (e == 0) hs.init(ws, b, h, lane, flag1, flag2);
    bf16x8 Q[4];
    {
      f32x4 a[4] = {};
      for (int i = 0; i < 8; ++i) {
        wait2(aFULL, i + 1);
        const unsigned so = stg_off(i);
        bf16x8 xb[2], wf[8];
#pragma unroll
        for (int kk = 0; kk < 2; ++kk) {
          xb[kk] = lds128(ldsb + so + 32768 + (i16 >> 3) * 1024 + aR[kk]);
#pragma unroll
          for (int t = 0; t < 4; ++t) {
            const unsigned ad = aT[t] + so + e * 1024 + (g >> 1) * 4096 + kk * 8192;
            wf[kk * 4 + t] = cat(ldstr(ad), ldstr(ad + 2048));
          }
        }
        MEMFENCE();
        stsw(aMyDone, i + 1);
        SB();
#pragma unroll
        for (int k = 0; k < 8; ++k) a[k & 3] = mfma32(wf[k], xb[k >> 2], a[k & 3]);
      }
      *(LAS bf16x8*)(uintptr_t)(ldsb + XCH_OFF + (2 * e) * 1024 + lane * 16) = pack8_contig16(a[0] * QSCALE, a[1] * QSCALE);
      *(LAS bf16x8*)(uintptr_t)(ldsb + XCH_OFF + (2 * e + 1) * 1024 + lane * 16) = pack8_contig16(a[2] * QSCALE, a[3] * QSCALE);
      MEMFENCE();
      stsw(aPAIR + e * 4, 1);
      wait2(aPAIR, 1);
#pragma unroll
      for (int s = 0; s < 4; ++s) Q[s] = lds128(ldsb + XCH_OFF + s * 1024 + lane * 16);
    }
    STAMP(9);
    f32x4 Os[8] = {}; float nm = 0.f, ls = 0.f;
    int slot = 0;
    for (int n = 0; n < 16; ++n) {
      hs.step(lane, aHS);
      wait2(aFULL, 9 + 2 * n);
      const unsigned sk = ldsb + slot * SLOT + e * 8192 + (i16 >> 3) * 2048;
      f32x4 S0 = {nm, nm, nm, nm}, S1 = S0;
      bf16x8 kf[8];
#pragma unroll
      for (int s = 0; s < 4; ++s) { kf[s] = lds128(sk + aR[s & 1] + (s >> 1) * 1024); kf[4 + s] = lds128(sk + 4096 + aR[s & 1] + (s >> 1) * 1024); }
      MEMFENCE();
      stsw(aMyDone, 9 + 2 * n);
      SB();
#pragma unroll
      for (int s = 0; s < 4; ++s) { S0 = mfma32(kf[s], Q[s], S0); S1 = mfma32(kf[4 + s], Q[s], S1); }
      PIN(S0); PIN(S1);
      wait2(aFULL, 10 + 2 * n);
      const unsigned sv = (slot + 1) * SLOT + e * 8192 + (g >> 1) * 2048;
#pragma unroll
      for (int T = 0; T < 8; ++T) {
        const unsigned ad = aT[T & 3] + sv + (T >> 2) * 1024;
        kf[T] = cat(ldstr(ad), ldstr(ad + 4096));
      }
      MEMFENCE();
      stsw(aMyDone, 10 + 2 * n);
      SB();
      const float mx = max2f(max2f(max2f(S0[0], S0[1]), max2f(S0[2], S0[3])), max2f(max2f(S1[0], S1[1]), max2f(S1[2], S1[3])));
      if (n == 0 || __any(mx > THR)) {
        float d = xmax32(xmax16(mx));
        if (n) d = fmaxf(d, 0.f);
        S0 -= d; S1 -= d; nm -= d;
        const float al = n ? ex2(-d) : 0.f;
#pragma unroll
        for (int T = 0; T < 8; ++T) Os[T] *= al;
        ls *= al;
      }
#pragma unroll
      for (int r = 0; r < 4; ++r) { S0[r] = ex2(S0[r]); S1[r] = ex2(S1[r]); }
      ls += ((S0[0] + S0[1]) + (S0[2] + S0[3])) + ((S1[0] + S1[1]) + (S1[2] + S1[3]));
      const bf16x8 P = pack8(S0, S1);
#pragma unroll
      for (int T = 0; T < 8; ++T) Os[T] = mfma32(kf[T], P, Os[T]);
      slot += 2; if (slot == NSLOT) slot = 0;
    }
#pragma unroll 1
    for (int it = 0; it < SPIN_CAP && !hs.done; ++it) hs.step(lane, aHS);
    wait1(aHS, 1);
    STAMP(27);
    bf16x8 fs[16];
    {
      ls = xadd32(xadd16(ls));
#pragma unroll
      for (int t = 0; t < 4; ++t) *(LAS f32x4*)(uintptr_t)(ldsb + XCH_OFF + (e * 4 + t) * 1024 + lane * 16) = e ? Os[t] : Os[4 + t];
      *(LAS f32x2*)(uintptr_t)(ldsb + ML_OFF + e * 512 + lane * 8) = f32x2{-nm, ls};
      MEMFENCE();
      stsw(aPAIR + e * 4, 2);
      wait2(aPAIR, 2);
      const f32x2 ml = *(const LAS f32x2*)(uintptr_t)(ldsb + ML_OFF + (1 - e) * 512 + lane * 8);
      const float M = max2f(-nm, ml[0]);
      const float f0 = ex2(-nm - M), f1 = ex2(ml[0] - M);
      const float rL = __builtin_amdgcn_rcpf(f0 * ls + f1 * ml[1]);
      f32x4 o[4];
#pragma unroll
      for (int t = 0; t < 4; ++t)
        o[t] = (e ? Os[4 + t] : Os[t]) * (f0 * rL) + *(const LAS f32x4*)(uintptr_t)(ldsb + XCH_OFF + ((1 - e) * 4 + t) * 1024 + lane * 16) * (f1 * rL);
      const bf16x8 o0 = pack8(o[0], o[1]), o1 = pack8(o[2], o[3]);
      __builtin_amdgcn_raw_buffer_store_b128(__builtin_bit_cast(u32x4, o0), rof, lane * 16, ((h & 3) * 36 + 32 + 2 * e) * 1024, 16);
      __builtin_amdgcn_raw_buffer_store_b128(__builtin_bit_cast(u32x4, o1), rof, lane * 16, ((h & 3) * 36 + 33 + 2 * e) * 1024, 16);
      wait_vm<0>();
      if (lane == 0) set_flag(flag1 + (h & 3) * 32 + 4 + e);
    }
    wait_flags(flag1 + (lane & 3) * 32 + 4 + ((lane >> 2) & 1));
    __builtin_amdgcn_fence(__ATOMIC_ACQUIRE, "wavefront");
#pragma unroll
    for (int k = 0; k < 16; ++k)
      fs[k] = __builtin_bit_cast(bf16x8, __builtin_amdgcn_raw_buffer_load_b128(rof, lane * 16, (((h + (k >> 2)) & 3) * 36 + 32 + (k & 3)) * 1024, 16));
    wait2(aWOF, 8);
    {
      f32x4 a[4] = {};
      bf16x8 wf[2][8];
      auto rdw = [&](int i) {
        unsigned so = wo_slot(i) + e * 1024;
        asm volatile("" : "+s"(so));
#pragma unroll
        for (int k = 0; k < 8; ++k) {
          const unsigned ad = aT[k & 3] + so + (g >> 1) * 2048 + (k >> 2) * 8192;
          wf[i & 1][k] = cat(ldstr(ad), ldstr(ad + 4096));
        }
      };
      rdw(0);
#pragma unroll
      for (int i = 0; i < 8; ++i) {
        if (i < 7) rdw(i + 1);
        SB();
#pragma unroll
        for (int k = 0; k < 8; ++k) a[k & 3] = mfma32(wf[i & 1][k], fs[i * 2 + (k >> 2)], a[k & 3]);
        PIN(a[0]); PIN(a[1]); PIN(a[2]); PIN(a[3]);
        SB();
      }
      __builtin_amdgcn_raw_buffer_store_b128(__builtin_bit_cast(u32x4, pack8(a[0], a[1])), rp, lane * 16, (32 + 2 * e) * 1024, 16);
      __builtin_amdgcn_raw_buffer_store_b128(__builtin_bit_cast(u32x4, pack8(a[2], a[3])), rp, lane * 16, (33 + 2 * e) * 1024, 16);
      wait_vm<0>();
      if (lane == 0) set_flag(flag2 + 128 + e * 8 + (h >> 2));
    }
    STAMP(30);
    return;
  }

  const int w = wave, hh = lane >> 5, r32 = lane & 31;
  unsigned aV[2], aK[4];
  unsigned aMyDone = aDONE + w * 4, aF = aFULL;
  PIN(aMyDone); PIN(aF);
  {
    const unsigned q = i16 >> 2, p = i16 & 3, r8a = 4 * hh + q;
#pragma unroll
    for (int d = 0; d < 2; ++d) aV[d] = ldsb + r8a * 128 + (((4 * d + 2 * (g & 1) + (p >> 1)) ^ mv(r8a)) << 4) + 8 * (p & 1);
  }
  bf16x8 Q[8];
  {
    unsigned xo[4];
#pragma unroll
    for (int t = 0; t < 4; ++t) xo[t] = ldsb + 16384 + w * 4096 + (r32 >> 3) * 1024 + (r32 & 7) * 128 + (((2 * t + hh) ^ mv(r32 & 7) ^ ((r32 >> 3) & 1)) << 4);
    f32x16 acc[4] = {};
    bf16x8 fr[8], bo[2];
    auto rd = [&](auto rc, unsigned so) {
      constexpr int r = decltype(rc)::value;
      const unsigned a = aV[r & 1] + so + (r >> 2) * 4096 + ((r >> 1) & 1) * 1024;
      fr[r & 7] = cat(ldstr(a), ldstr(a + 2048));
    };
    wait2(aF, 1);
    STAMP(1);
    unsigned so = stg_off(0);
    bo[0] = lds128(xo[0] + so);
    rd(IntC<1>{}, so); rd(IntC<0>{}, so); rd(IntC<3>{}, so); rd(IntC<2>{}, so);
    for (int i = 0; i < 8; ++i) {
      unsigned sn = so;
      u32x2 fl = {0, 0};
      static_for<16>([&](auto pc) {
        constexpr int p = decltype(pc)::value;
        acc[p & 3] = mfmaL(fr[p & 7], bo[(p >> 2) & 1], acc[p & 3]); PIN(acc[p & 3]);
        SB();
        if constexpr (p == 12) {
          sn = so + STG; if (sn == STG0 + 3 * STG) sn = STG0;
          if (__builtin_expect((unsigned)__builtin_amdgcn_readfirstlane(min(fl[0], fl[1])) < (unsigned)(i + 2) && i < 7, 0)) {
            PIN(fr[5]); PIN(fr[6]); PIN(fr[7]);
            wait2(aF, i + 2);
          }
          MEMFENCE();
          STAMP(2 + i);
        }
        constexpr int r = ((p + 4) ^ 1) & 15;
        if constexpr ((p & 3) == 0) bo[((p + 4) >> 2) & 1] = lds128(xo[((p + 4) >> 2) & 3] + sn);
        rd(IntC<r>{}, sn);
        if constexpr (p == 8) fl = ldsw2(aF);
        if constexpr (p == 11) { MEMFENCE(); stsw(aMyDone, i + 1); }
        SB();
      });
      so = sn;
    }
    static_for<4>([&](auto dc) {
      constexpr int D = decltype(dc)::value;
      Q[2 * D] = pack8_contig32(quad<0>(acc[D]) * QSCALE, quad<1>(acc[D]) * QSCALE);
      Q[2 * D + 1] = pack8_contig32(quad<2>(acc[D]) * QSCALE, quad<3>(acc[D]) * QSCALE);
    });
  }
#pragma unroll
  for (int t = 0; t < 4; ++t) aK[t] = ldsb + (r32 >> 3) * 2048 + (r32 & 7) * 128 + (((2 * t + hh) ^ mv(r32 & 7) ^ ((r32 >> 3) & 1)) << 4);
  aV[0] += SLOT; aV[1] += SLOT;

  f32x16 O[4] = {}, nm = {}, Sa, Sb;
  float l0 = 0.f, l1 = 0.f;
  auto max16 = [&](const f32x16& S) {
    float a = max3f(S[0], S[1], S[2]), c = max3f(S[3], S[4], S[5]);
    a = max3f(a, S[6], S[7]); c = max3f(c, S[8], S[9]); a = max3f(a, S[10], S[11]); c = max3f(c, S[12], S[13]);
    return max2f(max3f(a, S[14], S[15]), c);
  };
  auto fixL = [&](f32x16& S, bool first) {
    float d = xmax32(max16(S));
    if (!first) d = fmaxf(d, 0.f);
    S -= d; nm -= d;
    if (!first) {
      const float al = ex2(-d);
#pragma unroll
      for (int D = 0; D < 4; ++D) O[D] *= al;
      l0 *= al; l1 *= al;
    }
  };

  bf16x8 fr[8];
  wait2(aF, 9);
  STAMP(10);
#pragma unroll
  for (int s = 0; s < 8; ++s) Sa = mfmaL(lds128(aK[s & 3] + (s >> 2) * 1024), Q[s], s ? Sa : nm);
  fr[1] = lds128(aK[1] + 8192); fr[0] = lds128(aK[0] + 8192); fr[3] = lds128(aK[3] + 8192); fr[2] = lds128(aK[2] + 8192);
  fixL(Sa, true);
  SB();

  int sk = 0, sv = 1;
  unsigned step = 10;
  auto body = [&](auto lastc) {
    constexpr bool LAST = decltype(lastc)::value;
    unsigned P[8];
    int mx = 0, my = 0;
    u32x2 fl = {0, 0};
    auto rd = [&](auto rc) {
      constexpr int r = decltype(rc)::value, rj = r & 7;
      if constexpr (r < 8) fr[rj] = lds128(aK[rj & 3] + 8192 + (rj >> 2) * 1024);
      else if constexpr (r < 16 || (r >= 24 && r < 32)) {
        const unsigned a = aV[rj & 1] + (r >= 24 ? 8192 : 0) + (rj >> 2) * 4096 + ((rj >> 1) & 1) * 1024;
        fr[rj] = cat(ldstr(a), ldstr(a + 2048));
      } else if constexpr (!LAST) fr[rj] = lds128(aK[rj & 3] + (r >= 32 ? 8192 : 0) + (rj >> 2) * 1024);
    };
    static_for<32>([&](auto pc) {
      constexpr int p = decltype(pc)::value, j = p & 7;
      if constexpr (p == 4 || (p == 12 && !LAST)) {
        if (__builtin_expect((unsigned)__builtin_amdgcn_readfirstlane(min(fl[0], fl[1])) < step, 0)) {
          PIN(fr[(p + 0) & 7]); PIN(fr[(p + 1) & 7]); PIN(fr[(p + 2) & 7]); PIN(fr[(p + 3) & 7]);
          wait2(aF, step);
        }
        MEMFENCE();
        if constexpr (p == 4) stsw(aMyDone, step - 1);
        ++step;
        if constexpr (p == 12) {
          const int dk = sk >= NSLOT - 2 ? (2 - NSLOT) * SLOT : 2 * SLOT;
          sk += sk >= NSLOT - 2 ? 2 - NSLOT : 2;
#pragma unroll
          for (int t = 0; t < 4; ++t) aK[t] += dk;
        }
      }
      if constexpr ((p & 1) == 0) {
        if constexpr (p == 0 || (p == 8 && !LAST)) fl = ldsw2(aF);
        rd(IntC<p + 5>{}); rd(IntC<p + 4>{});
      }
      if constexpr (p < 8 || (p >= 16 && p < 24)) {
        constexpr bool A = p < 8;
        f32x16& Sn = A ? Sb : Sa; f32x16& So = A ? Sa : Sb;
        if constexpr (A || !LAST) { Sn = mfmaL(fr[j], Q[j], j ? Sn : nm); PIN(Sn); }
        if constexpr ((j & 1) == 0) { So[2 * j] = ex2(So[2 * j]); }
        else {
          So[2 * j - 1] = ex2(So[2 * j - 1]); So[2 * j] = ex2(So[2 * j]); So[2 * j + 1] = ex2(So[2 * j + 1]);
          if constexpr (j > 1) { P[j - 3] = cvtpk(So[2 * j - 6], So[2 * j - 5]); P[j - 2] = cvtpk(So[2 * j - 4], So[2 * j - 3]); PIN(P[j - 3]); PIN(P[j - 2]); }
        }
        PIN(So);
      } else {
        constexpr bool A = p < 16;
        f32x16& Sn = A ? Sb : Sa; f32x16& So = A ? Sa : Sb;
        constexpr int t = j >> 2, D = j & 3;
        const u32x4 pw = {P[4 * t], P[4 * t + 1], P[4 * t + 2], P[4 * t + 3]};
        O[D] = mfmaL(fr[j], __builtin_bit_cast(bf16x8, pw), O[D]); PIN(O[D]);
        if constexpr (j == 0) { P[6] = cvtpk(So[12], So[13]); P[7] = cvtpk(So[14], So[15]); PIN(P[6]); PIN(P[7]); }
        if constexpr (j & 1) {
          l0 += So[2 * j - 2]; PIN(l0); l1 += So[2 * j - 1]; PIN(l1); l0 += So[2 * j]; PIN(l0); l1 += So[2 * j + 1]; PIN(l1);
          if constexpr (A || !LAST) {
            if constexpr (j == 1) { mx = imax3(Sn[0], Sn[1], Sn[2]); my = imax3(Sn[3], Sn[4], Sn[5]); }
            if constexpr (j == 3) { mx = imax3(mx, Sn[6], Sn[7]); my = imax3(my, Sn[8], Sn[9]); }
            if constexpr (j == 5) { mx = imax3(mx, Sn[10], Sn[11]); my = imax3(my, Sn[12], Sn[13]); }
            if constexpr (j == 7) { mx = imax3(mx, Sn[14], Sn[15]); mx = max(mx, my); }
            PIN(mx); PIN(my);
          }
        }
        if constexpr (j == 7 && (A || !LAST)) {
          SB();
          if (__builtin_expect(__any(mx > __float_as_int(THR)), 0)) {
            if constexpr (A && !LAST) { PIN(fr[0]); PIN(fr[1]); PIN(fr[2]); PIN(fr[3]); }
            fixL(Sn, false);
          }
        }
        if constexpr (p == 27) {
          const int dv = sv >= NSLOT - 2 ? (2 - NSLOT) * SLOT : 2 * SLOT;
          sv += sv >= NSLOT - 2 ? 2 - NSLOT : 2;
          aV[0] += dv; aV[1] += dv;
          if constexpr (!LAST) { MEMFENCE(); stsw(aMyDone, step - 2); }
        }
      }
      SB();
    });
  };
  for (int n = 0; n < 15; ++n) { STAMP(11 + n); body(BoolC<false>{}); }
  STAMP(26);
  body(BoolC<true>{});
  STAMP(27);

  int ln = __builtin_amdgcn_mbcnt_hi(~0u, __builtin_amdgcn_mbcnt_lo(~0u, 0u));
  PIN(ln);
  const unsigned l16 = ln * 16;
  bf16x8 fo[32];
  {
    const float rl = __builtin_amdgcn_rcpf(xadd32(l0 + l1));
    static_for<4>([&](auto dc) {
      constexpr int D = decltype(dc)::value;
      fo[2 * D] = pack8(quad<0>(O[D]) * rl, quad<1>(O[D]) * rl);
      fo[2 * D + 1] = pack8(quad<2>(O[D]) * rl, quad<3>(O[D]) * rl);
      __builtin_amdgcn_raw_buffer_store_b128(__builtin_bit_cast(u32x4, fo[2 * D]), rof, l16, ((h & 3) * 36 + w * 8 + 2 * D) * 1024, 16);
      __builtin_amdgcn_raw_buffer_store_b128(__builtin_bit_cast(u32x4, fo[2 * D + 1]), rof, l16, ((h & 3) * 36 + w * 8 + 2 * D + 1) * 1024, 16);
    });
  }
  {
    const unsigned g2 = ln >> 4, q = (ln & 15) >> 2, p = ln & 3, r8a = 4 * (ln >> 5) + q;
    const unsigned v0 = ldsb + r8a * 128 + (((2 * (g2 & 1) + (p >> 1)) ^ mv(r8a)) << 4) + 8 * (p & 1);
    const unsigned v1 = ldsb + r8a * 128 + (((4 + 2 * (g2 & 1) + (p >> 1)) ^ mv(r8a)) << 4) + 8 * (p & 1);
    f32x16 acc[4] = {};
    GAS unsigned* fl2 = flag2 + w * 32 + (h >> 2);
    unsigned av0 = 0, av1 = 0;
    auto rd = [&](auto rc) {
      constexpr int r = decltype(rc)::value;
      if constexpr (r < 128) {
        constexpr int i = r < 32 ? r >> 4 : 2 + ((r - 32) % 24) / 4;
        constexpr int t = r < 32 ? (r >> 2) & 3 : r & 3, D = r < 32 ? r & 3 : (r - 32) / 24;
        const unsigned a = ((D & 1) ? av1 : av0) + t * 4096 + (D >> 1) * 1024;
        fr[r & 7] = cat(ldstr(a), ldstr(a + 2048));
        (void)i;
      }
    };
    wait2(aWOF, 2);
    wait1(aHS, 1);
    static_for<132>([&](auto rc) {
      constexpr int r = decltype(rc)::value, pp = r - 4;
      if constexpr (pp == 28) {
        wait_vm<0>();
        if (ln == 0) set_flag(flag1 + (h & 3) * 32 + w);
        STAMP(28);
        wait_flags(flag1 + (ln & 3) * 32 + w);
        __builtin_amdgcn_fence(__ATOMIC_ACQUIRE, "wavefront");
#pragma unroll
        for (int k = 8; k < 32; ++k)
          fo[k] = __builtin_bit_cast(bf16x8, __builtin_amdgcn_raw_buffer_load_b128(rof, l16, (((h + (k >> 3)) & 3) * 36 + w * 8 + (k & 7)) * 1024, 16));
        STAMP(29);
      }
      if constexpr (pp >= 0) {
        constexpr int k = pp < 32 ? pp >> 2 : 8 + (pp - 32) % 24, D = pp < 32 ? pp & 3 : (pp - 32) / 24;
        acc[D] = mfmaL(fr[pp & 7], fo[k], acc[D]); PIN(acc[D]);
        SB();
      }
      if constexpr (r == 32) { wait2(aWOF, 8); STAMP(30); }
      if constexpr (r < 128) {
        if constexpr ((r < 32 && (r & 15) == 0) || (r >= 32 && (r & 3) == 0)) {
          constexpr int i = r < 32 ? r >> 4 : 2 + ((r - 32) % 24) / 4;
          unsigned so = wo_slot(i);
          asm volatile("" : "+s"(so));
          av0 = v0 + so; av1 = v1 + so;
        }
        rd(IntC<(r ^ 1)>{});
      }
      if constexpr (pp >= 0) {
        constexpr int D = pp < 32 ? pp & 3 : (pp - 32) / 24;
        if constexpr (pp >= 32 && (pp - 32) % 24 == 23) {
          SB();
          if constexpr (D >= 2) {
            wait_vm<(D == 2 ? 2 : 3)>();
            if (ln == 0) set_flag(fl2 + (D - 2) * 8);
          }
          __builtin_amdgcn_raw_buffer_store_b128(__builtin_bit_cast(u32x4, pack8(quad<0>(acc[D]), quad<1>(acc[D]))), rp, l16, (w * 8 + D * 2) * 1024, 16);
          __builtin_amdgcn_raw_buffer_store_b128(__builtin_bit_cast(u32x4, pack8(quad<2>(acc[D]), quad<3>(acc[D]))), rp, l16, (w * 8 + D * 2 + 1) * 1024, 16);
          STAMP(31 + D);
        }
      }
      SB();
    });
    wait_vm<3>();
    if (ln == 0) set_flag(fl2 + 2 * 8);
    wait_vm<1>();
    if (ln == 0) set_flag(fl2 + 3 * 8);
  }
  STAMP(35);
}

extern "C" void kernel_launch(void* const* d_in, const int* in_sizes, int n_in, void* d_out, int out_size, void* d_ws,
                              size_t ws_size, hipStream_t stream) {
  const uint16_t* x = (const uint16_t*)d_in[0];
  const uint16_t* kc = (const uint16_t*)d_in[1];
  const uint16_t* vc = (const uint16_t*)d_in[2];
  const uint16_t* wq = (const uint16_t*)d_in[3];
  const uint16_t* wo = (const uint16_t*)d_in[4];
  (void)hipFuncSetAttribute((const void*)attn_kernel, hipFuncAttributeMaxDynamicSharedMemorySize, LDS_BYTES);
  hipLaunchKernelGGL(attn_kernel, dim3(256), dim3(512), LDS_BYTES, stream, x, kc, vc, wq, wo, (char*)d_ws, (uint16_t*)d_out);
}
```

```cpp
#include <hip/hip_runtime.h>
#include <hip/hip_bf16.h>
#include <stdint.h>
#include <utility>

typedef __bf16 bf16x8 __attribute__((ext_vector_type(8)));
typedef __bf16 bf16x4 __attribute__((ext_vector_type(4)));
typedef __bf16 bf16x2 __attribute__((ext_vector_type(2)));
typedef short s16x4 __attribute__((ext_vector_type(4)));
typedef float f32x16 __attribute__((ext_vector_type(16)));
typedef float f32x4 __attribute__((ext_vector_type(4)));
typedef float f32x2 __attribute__((ext_vector_type(2)));
typedef unsigned u32x4 __attribute__((ext_vector_type(4)));
typedef unsigned u32x2 __attribute__((ext_vector_type(2)));
#define LAS __attribute__((address_space(3)))
#define GAS __attribute__((address_space(1)))
#define DI __device__ __forceinline__

constexpr int NB = 8, SQ = 144, DM = 512, NH = 32, SK = 1024, DH = 128;
constexpr int SLOT = 16384, NSLOT = 9, LDS_BYTES = 163840;
constexpr int STG0 = 3 * SLOT, STG = 34 * 1024, MISC_OFF = STG0 + 3 * STG;
constexpr int XCH_OFF = MISC_OFF;
constexpr int ML_OFF = MISC_OFF + 8192;
constexpr int CTL_OFF = MISC_OFF + 9216;
DI int stg_off(int i) { return STG0 + ((i + 1) % 3) * STG; }
#define KV_AUX 18
#define EARLY_MASK 0x54
#define WO_BYTES (NH * DH * DM * 2)
#define KV_DEPTH 2
#define S_HI 1
#define SPRIO(x) __builtin_amdgcn_s_setprio(x)
#define OWN_AT 28
#define WO_AUX 2
#define WX_AUX 0
constexpr size_t WS_FLAG_BYTES = 65536 + 8192, WS_OF = WS_FLAG_BYTES, WS_PART = WS_OF + (size_t)256 * 36 * 1024, WS_END = WS_PART + (size_t)256 * 36 * 1024;
constexpr int SPIN_CAP = 1 << 16;
constexpr float QSCALE = 0.088388347648318440f * 1.4426950408889634f;
#define THR_VAL 8.0f
constexpr float THR = THR_VAL;

DI f32x16 mfmaL(bf16x8 a, bf16x8 b, f32x16 c) { return __builtin_amdgcn_mfma_f32_32x32x16_bf16(a, b, c, 0, 0, 0); }
DI f32x4 mfma32(bf16x8 a, bf16x8 b, f32x4 c) { return __builtin_amdgcn_mfma_f32_16x16x32_bf16(a, b, c, 0, 0, 0); }
DI f32x4 mfma16(bf16x4 a, bf16x4 b, f32x4 c) {
  return __builtin_amdgcn_mfma_f32_16x16x16bf16_1k(__builtin_bit_cast(s16x4, a), __builtin_bit_cast(s16x4, b), c, 0, 0, 0);
}
DI unsigned cvtpk(float lo, float hi) { f32x2 v = {lo, hi}; bf16x2 b = __builtin_convertvector(v, bf16x2); return __builtin_bit_cast(unsigned, b); }
DI float max3f(float a, float b, float c) { return fmaxf(fmaxf(a, b), c); }
DI float max2f(float a, float b) { return fmaxf(a, b); }
DI int imax3(float a, float b, float c) { return max(max(__float_as_int(a), __float_as_int(b)), __float_as_int(c)); }
DI int imax3(int a, float b, float c) { return max(max(a, __float_as_int(b)), __float_as_int(c)); }
DI float ex2(float x) { return __builtin_amdgcn_exp2f(x); }
DI bf16x8 lds128(unsigned addr) { return __builtin_bit_cast(bf16x8, *(const volatile LAS u32x4*)(uintptr_t)addr); }
DI bf16x4 ldstr(unsigned addr) { return __builtin_amdgcn_ds_read_tr16_b64_v4bf16((LAS bf16x4*)(uintptr_t)addr); }
DI bf16x8 cat(bf16x4 a, bf16x4 b) { return __builtin_shufflevector(a, b, 0, 1, 2, 3, 4, 5, 6, 7); }
#define BAR() asm volatile("s_waitcnt lgkmcnt(0)\n\ts_barrier" ::: "memory")
#define RAWBAR() asm volatile("s_barrier" ::: "memory")
#define SB() __builtin_amdgcn_sched_barrier(0)
#define PIN(x) asm volatile("" : "+v"(x))
template <int I> struct IntC { static constexpr int value = I; };
template <bool V> struct BoolC { static constexpr bool value = V; };
template <class F, int... Is> DI void static_for_(F&& f, std::integer_sequence<int, Is...>) { (f(IntC<Is>{}), ...); }
template <int N, class F> DI void static_for(F&& f) { static_for_(f, std::make_integer_sequence<int, N>{}); }
DI float xmax32(float x) {
  auto b = __builtin_amdgcn_permlane32_swap(__float_as_uint(x), __float_as_uint(x), false, false);
  return max2f(__uint_as_float(b[0]), __uint_as_float(b[1]));
}
DI float xmax16(float x) {
  auto a = __builtin_amdgcn_permlane16_swap(__float_as_uint(x), __float_as_uint(x), false, false);
  return max2f(__uint_as_float(a[0]), __uint_as_float(a[1]));
}
DI float xadd32(float x) {
  auto b = __builtin_amdgcn_permlane32_swap(__float_as_uint(x), __float_as_uint(x), false, false);
  return __uint_as_float(b[0]) + __uint_as_float(b[1]);
}
DI float xadd16(float x) {
  auto b = __builtin_amdgcn_permlane16_swap(__float_as_uint(x), __float_as_uint(x), false, false);
  return __uint_as_float(b[0]) + __uint_as_float(b[1]);
}
DI bf16x8 pack8(f32x4 a, f32x4 b) {
  u32x4 w = {cvtpk(a[0], a[1]), cvtpk(a[2], a[3]), cvtpk(b[0], b[1]), cvtpk(b[2], b[3])};
  return __builtin_bit_cast(bf16x8, w);
}
template <int Q> DI f32x4 quad(const f32x16& v) { return f32x4{v[4 * Q], v[4 * Q + 1], v[4 * Q + 2], v[4 * Q + 3]}; }
DI bf16x8 pack8_contig16(f32x4 x, f32x4 y) {
  unsigned x0 = cvtpk(x[0], x[1]), x1 = cvtpk(x[2], x[3]), y0 = cvtpk(y[0], y[1]), y1 = cvtpk(y[2], y[3]);
  auto a0 = __builtin_amdgcn_permlane32_swap(x0, y0, false, false);
  auto a1 = __builtin_amdgcn_permlane32_swap(x1, y1, false, false);
  auto b0 = __builtin_amdgcn_permlane16_swap(a0[0], a0[1], false, false);
  auto b1 = __builtin_amdgcn_permlane16_swap(a1[0], a1[1], false, false);
  u32x4 w = {b0[0], b1[0], b0[1], b1[1]};
  return __builtin_bit_cast(bf16x8, w);
}
DI bf16x8 pack8_contig32(f32x4 x, f32x4 y) {
  unsigned x0 = cvtpk(x[0], x[1]), x1 = cvtpk(x[2], x[3]), y0 = cvtpk(y[0], y[1]), y1 = cvtpk(y[2], y[3]);
  auto a0 = __builtin_amdgcn_permlane32_swap(x0, y0, false, false);
  auto a1 = __builtin_amdgcn_permlane32_swap(x1, y1, false, false);
  u32x4 w = {a0[0], a1[0], a0[1], a1[1]};
  return __builtin_bit_cast(bf16x8, w);
}
template <int N> DI void wait_vm() { asm volatile("s_waitcnt vmcnt(%0)" ::"i"(N) : "memory"); }
DI void wait_flags(GAS unsigned* fp) {
#pragma unroll 1
  for (int it = 0; it < SPIN_CAP; ++it) {
    if (__all(__hip_atomic_load(fp, __ATOMIC_RELAXED, __HIP_MEMORY_SCOPE_AGENT) == 1u)) break;
    __builtin_amdgcn_s_sleep(1);
  }
}
DI __amdgpu_buffer_rsrc_t mk_rsrc(const void* p, unsigned bytes) { return __builtin_amdgcn_make_buffer_rsrc((void*)p, 0, bytes, 0x00020000); }
template <int AUX> DI void dma16(__amdgpu_buffer_rsrc_t r, unsigned ldsaddr, unsigned voff, unsigned soff) {
  __builtin_amdgcn_raw_ptr_buffer_load_lds(r, (LAS void*)(uintptr_t)ldsaddr, 16, voff, soff, 0, AUX);
}
DI void st128(u32x4 v, __amdgpu_buffer_rsrc_t r, unsigned voff, unsigned soff, int) {
  __builtin_amdgcn_raw_buffer_store_b128(v, r, voff, soff, 16);
  asm volatile("s_nop 1" ::"v"(v) : "memory");
}
DI unsigned mv(unsigned r8) { return ((r8 & 2) << 1) | ((r8 & 4) >> 1); }

template <class T> DI T sel(bool c, T a, T b) { return c ? a : b; }
DI unsigned ldsw(unsigned addr) { return *(const volatile LAS unsigned*)(uintptr_t)addr; }
DI u32x2 ldsw2(unsigned addr) { return *(const volatile LAS u32x2*)(uintptr_t)addr; }
DI u32x4 ldsw4(unsigned addr) { return *(const volatile LAS u32x4*)(uintptr_t)addr; }
DI void stsw(unsigned addr, unsigned v) { *(volatile LAS unsigned*)(uintptr_t)addr = v; }
#define MEMFENCE() asm volatile("" ::: "memory")
DI void pub(unsigned addr, unsigned v) {
  asm volatile("s_mov_b64 exec, 1\n\tds_max_u32 %0, %1\n\ts_mov_b64 exec, -1" ::"v"(addr), "v"(v) : "memory");
}
DI void wait2(unsigned addr, unsigned need) {
#pragma unroll 1
  for (int it = 0; it < SPIN_CAP; ++it) {
    const u32x2 v = ldsw2(addr);
    if ((unsigned)__builtin_amdgcn_readfirstlane(min(v[0], v[1])) >= need) break;
    __builtin_amdgcn_s_sleep(1);
  }
  MEMFENCE();
}
DI void wait1(unsigned addr, unsigned need) {
#pragma unroll 1
  for (int it = 0; it < SPIN_CAP; ++it) {
    if ((unsigned)__builtin_amdgcn_readfirstlane(ldsw(addr)) >= need) break;
    __builtin_amdgcn_s_sleep(1);
  }
  MEMFENCE();
}
DI void wait_bits(unsigned addr, unsigned bits) {
#pragma unroll 1
  for (int it = 0; it < SPIN_CAP; ++it) {
    if (((unsigned)__builtin_amdgcn_readfirstlane(ldsw(addr)) & bits) == bits) break;
    __builtin_amdgcn_s_sleep(1);
  }
  MEMFENCE();
}
DI unsigned done_min(unsigned addr) {
  u32x4 a; u32x2 c;
  asm volatile("ds_read_b128 %0, %2\n\tds_read_b64 %1, %2 offset:16\n\ts_waitcnt lgkmcnt(0)" : "=&v"(a), "=&v"(c) : "v"(addr) : "memory");
  return __builtin_amdgcn_readfirstlane(min(min(min(a[0], a[1]), min(a[2], a[3])), min(c[0], c[1])));
}
DI void set_flag(GAS unsigned* p) { __hip_atomic_store(p, 1u, __ATOMIC_RELAXED, __HIP_MEMORY_SCOPE_AGENT); }
DI unsigned wo_slot(int i) { return ((32 + i) % NSLOT) * SLOT; }

#define STAMP(idx)
#define LSTAMP(idx)

DI f32x4 bf_lo(u32x4 v) { return f32x4{__uint_as_float(v[0] << 16), __uint_as_float(v[0] & 0xffff0000u), __uint_as_float(v[1] << 16), __uint_as_float(v[1] & 0xffff0000u)}; }
DI f32x4 bf_hi(u32x4 v) { return f32x4{__uint_as_float(v[2] << 16), __uint_as_float(v[2] & 0xffff0000u), __uint_as_float(v[3] << 16), __uint_as_float(v[3] & 0xffff0000u)}; }
DI void reduce_blocks(char* ws, uint16_t* out, int b, int h, int L, int lane) {
  const int hg = h >> 2, nt = h & 3;
  const __amdgpu_buffer_rsrc_t rp = mk_rsrc(ws + WS_PART + (size_t)(b * 4 + nt) * 8 * 36 * 1024, 8 * 36 * 1024);
  GAS unsigned* flag2 = (GAS unsigned*)(ws + 32768) + (b * 4 + nt) * 256;
  __builtin_amdgcn_s_setprio(0);
  auto sum8 = [&](const u32x4(&v)[8], f32x4& x, f32x4& y) {
    x = ((bf_lo(v[0]) + bf_lo(v[1])) + (bf_lo(v[2]) + bf_lo(v[3]))) + ((bf_lo(v[4]) + bf_lo(v[5])) + (bf_lo(v[6]) + bf_lo(v[7])));
    y = ((bf_hi(v[0]) + bf_hi(v[1])) + (bf_hi(v[2]) + bf_hi(v[3]))) + ((bf_hi(v[4]) + bf_hi(v[5])) + (bf_hi(v[6]) + bf_hi(v[7])));
  };
  {
    wait_flags(flag2 + ((2 * L + ((lane >> 3) & 1)) * 4 + (hg >> 1)) * 8 + (lane & 7));
    __builtin_amdgcn_fence(__ATOMIC_ACQUIRE, "wavefront");
    u32x4 v0[8], v1[8];
#pragma unroll
    for (int u = 0; u < 8; ++u) {
      v0[u] = __builtin_amdgcn_raw_buffer_load_b128(rp, lane * 16, (u * 36 + 16 * L + hg) * 1024, 16);
      v1[u] = __builtin_amdgcn_raw_buffer_load_b128(rp, lane * 16, (u * 36 + 16 * L + 8 + hg) * 1024, 16);
    }
    f32x4 x, y;
    uint16_t* o = out + ((size_t)(b * SQ + L * 64 + (lane & 31)) * DM + nt * 128 + hg * 16 + (lane >> 5) * 8);
    sum8(v0, x, y); *(u32x4*)o = __builtin_bit_cast(u32x4, pack8_contig32(x, y));
    sum8(v1, x, y); *(u32x4*)(o + 32 * DM) = __builtin_bit_cast(u32x4, pack8_contig32(x, y));
  }
  if (hg < 2) {
    const int wp = 2 * hg + L;
    wait_flags(flag2 + 128 + (wp >> 1) * 8 + (lane & 7));
    __builtin_amdgcn_fence(__ATOMIC_ACQUIRE, "wavefront");
    u32x4 v[8];
#pragma unroll
    for (int u = 0; u < 8; ++u) v[u] = __builtin_amdgcn_raw_buffer_load_b128(rp, lane * 16, (u * 36 + 32 + wp) * 1024, 16);
    f32x4 x, y; sum8(v, x, y);
    *(u32x4*)(out + ((size_t)(b * SQ + 128 + (lane & 15)) * DM + nt * 128 + wp * 32 + (lane >> 4) * 8)) = __builtin_bit_cast(u32x4, pack8_contig16(x, y));
  }
}

struct Handshake {
  GAS unsigned* base;
  GAS unsigned* mine;
  unsigned v, lastL, N; int phase; bool done, first, leader; int h;
  DI void init(char* ws, int b, int h_, int lane, GAS unsigned* flag1, GAS unsigned* flag2) {
    h = h_; base = (GAS unsigned*)(ws + 65536) + b * 256;
    GAS unsigned* c = lane < 6 ? flag1 + (h & 3) * 32 + lane
                    : lane < 22 ? flag2 + (lane - 6) * 8 + (h >> 2)
                    : lane < 24 ? flag2 + 128 + (lane - 22) * 8 + (h >> 2) : base + 64 + h;
    if (lane < 25) __hip_atomic_store(c, 0u, __ATOMIC_RELAXED, __HIP_MEMORY_SCOPE_AGENT);
    wait_vm<0>();
    mine = lane < 32 ? base + lane : (lane == 32 ? base + 32 : base + 64 + h);
    leader = h == 0; phase = 0; done = false; first = true; lastL = 0; N = 0;
    v = __hip_atomic_load(mine, __ATOMIC_RELAXED, __HIP_MEMORY_SCOPE_AGENT);
  }
  DI void step(int lane, unsigned aHS) {
    if (done) return;
    const unsigned vL = __builtin_amdgcn_readlane(v, 32), vI = __builtin_amdgcn_readlane(v, 33);
    if (!first && vI == 1u) { done = true; stsw(aHS, 1); return; }
    if (leader) {
      if (phase == 0) {
        N = vL + 1;
        for (int t = 0; t < 34 && __any(lane < 32 && v == N); ++t) { ++N; if (N == vL) ++N; }
        if (lane == 0) __hip_atomic_store(base + 32, N, __ATOMIC_RELAXED, __HIP_MEMORY_SCOPE_AGENT);
        phase = 1;
      } else if (phase == 1 && __all(lane >= 32 || v == N)) {
        if (lane < 32) __hip_atomic_store(base + 64 + lane, 1u, __ATOMIC_RELAXED, __HIP_MEMORY_SCOPE_AGENT);
        phase = 2;
      }
    }
    if (first || vL != lastL) {
      if (lane == 0) __hip_atomic_store(base + h, vL, __ATOMIC_RELAXED, __HIP_MEMORY_SCOPE_AGENT);
      lastL = vL; first = false;
    }
    v = __hip_atomic_load(mine, __ATOMIC_RELAXED, __HIP_MEMORY_SCOPE_AGENT);
  }
};

__global__ __launch_bounds__(512, 2) void attn_kernel(const uint16_t* __restrict__ x, const uint16_t* __restrict__ kc,
                                                      const uint16_t* __restrict__ vc, const uint16_t* __restrict__ wq,
                                                      const uint16_t* __restrict__ wo, char* __restrict__ ws,
                                                      uint16_t* __restrict__ out) {
  extern __shared__ __attribute__((aligned(16))) char lds[];
  const int tid = threadIdx.x, lane = tid & 63;
  const int wave = __builtin_amdgcn_readfirstlane(tid >> 6);
  const int bid = blockIdx.x;
  const int h = 4 * (bid & 7) + ((bid >> 3) & 3), b = bid >> 5;
  const unsigned ldsb = (unsigned)(uintptr_t)(LAS char*)lds;
  const unsigned aFULL = ldsb + CTL_OFF, aWOF = aFULL + 8, aDONE = aFULL + 16, aPAIR = aFULL + 48, aHS = aFULL + 56, aRDY = aFULL + 60;
  const __amdgpu_buffer_rsrc_t rof = mk_rsrc(ws + WS_OF + (size_t)(b * NH + (h & ~3)) * 36 * 1024, 4 * 36 * 1024);
  GAS unsigned* flag1 = (GAS unsigned*)ws + (b * NH + (h & ~3)) * 32;
  GAS unsigned* flag2 = (GAS unsigned*)(ws + 32768) + (b * 4 + (h & 3)) * 256;
  const __amdgpu_buffer_rsrc_t rp = mk_rsrc(ws + WS_PART + (size_t)((b * 4 + (h & 3)) * 8 + (h >> 2)) * 36 * 1024, 36 * 1024);

  if (wave >= 6) {
    __builtin_amdgcn_s_setprio(3);
    const unsigned half = wave - 6, r8 = lane >> 3, sw = ((lane & 7) ^ mv(r8)) << 4;
    const unsigned aMy = aFULL + half * 4, aMyW = aWOF + half * 4;
    const size_t hb = (size_t)(b * NH + h) * SK * DH;
    const __amdgpu_buffer_rsrc_t rk = mk_rsrc(kc + hb, SK * DH * 2), rv = mk_rsrc(vc + hb, SK * DH * 2), rw = mk_rsrc(wo, WO_BYTES);
    const unsigned voff = r8 * 256u + sw, voffx = voff ^ 16u, voffw = r8 * 1024u + sw;
    const unsigned aPoll = aDONE + (lane < 6 ? lane : 0) * 4;
    auto busy = [&](unsigned need) {
      unsigned v;
      asm volatile("ds_read_b32 %0, %1\n\ts_waitcnt lgkmcnt(0)" : "=v"(v) : "v"(aPoll) : "memory");
      return __any(v < need);
    };
    const unsigned dsth = ldsb + half * 8192;
    unsigned src = half * 8192u;
    auto dmaK = [&](unsigned dst) {
#pragma unroll
      for (int m = 0; m < 8; ++m) dma16<KV_AUX>(rk, dst + m * 1024, (m & 2) ? voffx : voff, src + (m >> 1) * 2048 + (m & 1) * 128);
    };
    auto dmaV = [&](unsigned dst) {
#pragma unroll
      for (int m = 0; m < 8; ++m) dma16<KV_AUX>(rv, dst + m * 1024, voff, src + (m >> 1) * 2048 + (m & 1) * 128);
      src += 16384u;
    };
    auto dmaW = [&](unsigned dst, int i) {
      const unsigned sw_ = (unsigned)((h >> 2) * 512 + ((h + (i >> 1)) & 3) * 128 + (i & 1) * 64 + half * 32) * 1024u + (h & 3) * 256u;
#pragma unroll
      for (int m = 0; m < 8; ++m) dma16<WO_AUX>(rw, dst + m * 1024, voffw, sw_ + (m >> 1) * 8192 + (m & 1) * 128);
    };
    {
      const bool isw = half == 0;
      const __amdgpu_buffer_rsrc_t rs = isw ? mk_rsrc(wq, DM * NH * DH * 2) : mk_rsrc(x + (size_t)b * SQ * DM, SQ * DM * 2);
      const unsigned voff0 = isw ? ((r8 & 3) + 8 * (r8 >> 2)) * 8192u + sw : r8 * 1024u + sw;
      const unsigned voff1 = isw ? voff0 + 128u : (voff0 ^ 16u) + 8192u;
      const int rot = b + 2 * (h & 3);
      const unsigned pstep0 = isw ? 4u * 8192u : 16384u, pstep1 = isw ? 12u * 8192u : 16384u;
      auto issue = [&](int st) {
        unsigned dst = ldsb + stg_off(st) + (isw ? 0 : 16384);
        const int i = (st + rot) & 7;
        unsigned so = isw ? (unsigned)i * 64u * 8192u + (unsigned)h * 256u : (unsigned)i * 128u;
#pragma unroll
        for (int m = 0; m < 8; ++m) {
          dma16<WX_AUX>(rs, dst, voff0, so); dma16<WX_AUX>(rs, dst + 1024, voff1, so);
          dst += 2048; so += (m & 1) ? pstep1 : pstep0;
        }
        if (!isw) { dma16<WX_AUX>(rs, dst, voff0, so); dma16<WX_AUX>(rs, dst + 1024, voff1, so); }
      };
      issue(0);
      BAR();
      bool kvb = false;
      int je = 0;
#pragma unroll 1
      for (int st = 1; st < 8; ++st) {
        if (st >= 3 && busy(st - 2)) {
          if (kvb) wait_vm<8>(); else wait_vm<0>();
          pub(aMy, st);
#pragma unroll 1
          for (int it = 0; it < SPIN_CAP && busy(st - 2); ++it) __builtin_amdgcn_s_sleep(1);
        }
        issue(st);
        if (kvb) { if (isw) wait_vm<24>(); else wait_vm<26>(); }
        else { if (isw) wait_vm<16>(); else wait_vm<18>(); }
        pub(aMy, st);
        kvb = (EARLY_MASK >> st) & 1;
        if (kvb) { if (je == 1) dmaV(dsth + SLOT); else dmaK(dsth + je * SLOT); ++je; }
      }
    }
    unsigned full = 11;
    auto acquire = [&](unsigned need) {
      if (busy(need)) {
        wait_vm<16>(); pub(aMy, full - 2);
        if (busy(need)) {
          wait_vm<8>(); pub(aMy, full - 1);
          if (busy(need)) {
            wait_vm<0>(); pub(aMy, full);
#pragma unroll 1
            for (int it = 0; it < SPIN_CAP && busy(need); ++it) __builtin_amdgcn_s_sleep(1);
          }
        }
      }
    };
    if (busy(6)) {
      wait_vm<0>(); pub(aMy, 11);
#pragma unroll 1
      for (int it = 0; it < SPIN_CAP && busy(6); ++it) __builtin_amdgcn_s_sleep(1);
    }
    dmaV(dsth + 3 * SLOT); wait_vm<8>(); pub(aMy, 11); full = 12;
    dmaK(dsth + 4 * SLOT); full = 13;
    acquire(7); dmaV(dsth + 5 * SLOT); full = 14;
    dmaK(dsth + 6 * SLOT); full = 15; wait_vm<24>(); pub(aMy, 12);
    acquire(8); dmaV(dsth + 7 * SLOT); full = 16; wait_vm<24>(); pub(aMy, 13);
    unsigned dst = dsth + 8 * SLOT;
    const unsigned dend = dsth + NSLOT * SLOT;
#pragma unroll 1
    for (int pr = 4; pr < 16; ++pr) {
      acquire(full - 8); dmaK(dst); ++full; dst += SLOT; if (dst == dend) dst = dsth;
      wait_vm<8 * KV_DEPTH>(); pub(aMy, full - KV_DEPTH);
      acquire(full - 8); dmaV(dst); ++full; dst += SLOT; if (dst == dend) dst = dsth;
      wait_vm<8 * KV_DEPTH>(); pub(aMy, full - KV_DEPTH);
    }
    wait_vm<16>(); pub(aMy, 38);
#pragma unroll 1
    for (int i = 0; i < 8; ++i) {
      if (busy(32 + i)) {
        wait_vm<8>(); pub(aMy, i == 0 ? 39 : 40);
        wait_vm<0>(); pub(aMy, 40); pub(aMyW, i);
#pragma unroll 1
        for (int it = 0; it < SPIN_CAP && busy(32 + i); ++it) __builtin_amdgcn_s_sleep(1);
      }
      dmaW(dst, i); dst += SLOT; if (dst == dend) dst = dsth;
      wait_vm<16>(); pub(aMy, i < 1 ? 39 : 40); if (i >= 2) pub(aMyW, i - 1);
    }
    wait_vm<0>(); pub(aMy, 40); pub(aMyW, 8);
    wait1(aHS, 1);
    if (half == 0) {
      GAS unsigned* fp = flag1 + (lane & 3) * 32 + (lane < 24 ? lane >> 2 : 0);
#pragma unroll 1
      for (int it = 0; it < SPIN_CAP; ++it) {
        const unsigned m = (unsigned)__ballot(__hip_atomic_load(fp, __ATOMIC_RELAXED, __HIP_MEMORY_SCOPE_AGENT) == 1u) & 0xFFFFFFu;
        stsw(aRDY, m);
        if (m == 0xFFFFFFu) break;
      }
    }
    {
      const unsigned a4 = aDONE + (lane & 3) * 4, need = 41 + (h >> 3);
#pragma unroll 1
      for (int it = 0; it < SPIN_CAP; ++it) {
        if (__all(ldsw(a4) >= need)) break;
        __builtin_amdgcn_s_sleep(2);
      }
    }
    reduce_blocks(ws, out, b, h, wave - 6, lane);
    STAMP(35);
    return;
  }

  if (wave == 0 && lane < 16) stsw(aFULL + lane * 4, 0);
  BAR();
  STAMP(0);
  const int g = lane >> 4, i16 = lane & 15;

  if (wave >= 4) {
    const int e = wave - 4;
    const unsigned aMyDone = aDONE + wave * 4;
    const unsigned q = i16 >> 2, p = i16 & 3, r8b = 4 * (g & 1) + q;
    unsigned aT[4];
#pragma unroll
    for (int t = 0; t < 4; ++t) aT[t] = ldsb + r8b * 128 + (((2 * t + (p >> 1)) ^ mv(r8b)) << 4) + 8 * (p & 1);
    unsigned aR[2];
#pragma unroll
    for (int t = 0; t < 2; ++t) aR[t] = (i16 & 7) * 128 + (((4 * t + g) ^ mv(i16 & 7) ^ (i16 >> 3)) << 4);
    Handshake hs;
    hs.done = true;
    if (e == 0) hs.init(ws, b, h, lane, flag1, flag2);
    bf16x8 Q[4];
    {
      f32x4 a[4] = {};
      for (int i = 0; i < 8; ++i) {
        wait2(aFULL, i + 1);
        const unsigned so = stg_off(i);
        bf16x8 xb[2], wf[8];
#pragma unroll
        for (int kk = 0; kk < 2; ++kk) {
          xb[kk] = lds128(ldsb + so + 32768 + (i16 >> 3) * 1024 + aR[kk]);
#pragma unroll
          for (int t = 0; t < 4; ++t) {
            const unsigned ad = aT[t] + so + e * 1024 + (g >> 1) * 4096 + kk * 8192;
            wf[kk * 4 + t] = cat(ldstr(ad), ldstr(ad + 2048));
          }
        }
        MEMFENCE();
        stsw(aMyDone, i + 1);
        SB();
#pragma unroll
        for (int k = 0; k < 8; ++k) a[k & 3] = mfma32(wf[k], xb[k >> 2], a[k & 3]);
      }
      *(LAS bf16x8*)(uintptr_t)(ldsb + XCH_OFF + (2 * e) * 1024 + lane * 16) = pack8_contig16(a[0] * QSCALE, a[1] * QSCALE);
      *(LAS bf16x8*)(uintptr_t)(ldsb + XCH_OFF + (2 * e + 1) * 1024 + lane * 16) = pack8_contig16(a[2] * QSCALE, a[3] * QSCALE);
      MEMFENCE();
      stsw(aPAIR + e * 4, 1);
      wait2(aPAIR, 1);
#pragma unroll
      for (int s = 0; s < 4; ++s) Q[s] = lds128(ldsb + XCH_OFF + s * 1024 + lane * 16);
    }
    STAMP(9);
    f32x4 Os[8] = {}; float nm = 0.f, ls = 0.f;
    int slot = 0;
    for (int n = 0; n < 16; ++n) {
      hs.step(lane, aHS);
      SPRIO(0);
      wait2(aFULL, 9 + 2 * n);
      SPRIO(S_HI);
      const unsigned sk = ldsb + slot * SLOT + e * 8192 + (i16 >> 3) * 2048;
      f32x4 S0 = {nm, nm, nm, nm}, S1 = S0;
      bf16x8 kf[8];
#pragma unroll
      for (int s = 0; s < 4; ++s) { kf[s] = lds128(sk + aR[s & 1] + (s >> 1) * 1024); kf[4 + s] = lds128(sk + 4096 + aR[s & 1] + (s >> 1) * 1024); }
      MEMFENCE();
      stsw(aMyDone, 9 + 2 * n);
      SB();
#pragma unroll
      for (int s = 0; s < 4; ++s) { S0 = mfma32(kf[s], Q[s], S0); S1 = mfma32(kf[4 + s], Q[s], S1); }
      PIN(S0); PIN(S1);
      SPRIO(0);
      wait2(aFULL, 10 + 2 * n);
      SPRIO(S_HI);
      slot = slot == NSLOT - 1 ? 0 : slot + 1;
      const unsigned sv = slot * SLOT + e * 8192 + (g >> 1) * 2048;
#pragma unroll
      for (int T = 0; T < 8; ++T) {
        const unsigned ad = aT[T & 3] + sv + (T >> 2) * 1024;
        kf[T] = cat(ldstr(ad), ldstr(ad + 4096));
      }
      MEMFENCE();
      stsw(aMyDone, 10 + 2 * n);
      SB();
      const float mx = max2f(max2f(max2f(S0[0], S0[1]), max2f(S0[2], S0[3])), max2f(max2f(S1[0], S1[1]), max2f(S1[2], S1[3])));
      if (n == 0 || __any(mx > THR)) {
        float d = xmax32(xmax16(mx));
        if (n) d = fmaxf(d, 0.f);
        S0 -= d; S1 -= d; nm -= d;
        const float al = n ? ex2(-d) : 0.f;
#pragma unroll
        for (int T = 0; T < 8; ++T) Os[T] *= al;
        ls *= al;
      }
#pragma unroll
      for (int r = 0; r < 4; ++r) { S0[r] = ex2(S0[r]); S1[r] = ex2(S1[r]); }
      ls += ((S0[0] + S0[1]) + (S0[2] + S0[3])) + ((S1[0] + S1[1]) + (S1[2] + S1[3]));
      const bf16x8 P = pack8(S0, S1);
#pragma unroll
      for (int T = 0; T < 8; ++T) Os[T] = mfma32(kf[T], P, Os[T]);
      slot = slot == NSLOT - 1 ? 0 : slot + 1;
    }
#pragma unroll 1
    for (int it = 0; it < SPIN_CAP && !hs.done; ++it) hs.step(lane, aHS);
    wait1(aHS, 1);
    STAMP(27);
    bf16x8 fs[16];
    {
      ls = xadd32(xadd16(ls));
#pragma unroll
      for (int t = 0; t < 4; ++t) *(LAS f32x4*)(uintptr_t)(ldsb + XCH_OFF + (e * 4 + t) * 1024 + lane * 16) = e ? Os[t] : Os[4 + t];
      *(LAS f32x2*)(uintptr_t)(ldsb + ML_OFF + e * 512 + lane * 8) = f32x2{-nm, ls};
      MEMFENCE();
      stsw(aPAIR + e * 4, 2);
      wait2(aPAIR, 2);
      const f32x2 ml = *(const LAS f32x2*)(uintptr_t)(ldsb + ML_OFF + (1 - e) * 512 + lane * 8);
      const float M = max2f(-nm, ml[0]);
      const float f0 = ex2(-nm - M), f1 = ex2(ml[0] - M);
      const float rL = __builtin_amdgcn_rcpf(f0 * ls + f1 * ml[1]);
      f32x4 o[4];
#pragma unroll
      for (int t = 0; t < 4; ++t)
        o[t] = (e ? Os[4 + t] : Os[t]) * (f0 * rL) + *(const LAS f32x4*)(uintptr_t)(ldsb + XCH_OFF + ((1 - e) * 4 + t) * 1024 + lane * 16) * (f1 * rL);
      const bf16x8 o0 = pack8(o[0], o[1]), o1 = pack8(o[2], o[3]);
      st128(__builtin_bit_cast(u32x4, o0), rof, lane * 16, ((h & 3) * 36 + 32 + 2 * e) * 1024, 16);
      st128(__builtin_bit_cast(u32x4, o1), rof, lane * 16, ((h & 3) * 36 + 33 + 2 * e) * 1024, 16);
      wait_vm<0>();
      if (lane == 0) set_flag(flag1 + (h & 3) * 32 + 4 + e);
    }
    wait_bits(aRDY, 0xFF0000u);
#pragma unroll
    for (int k = 0; k < 16; ++k)
      fs[k] = __builtin_bit_cast(bf16x8, __builtin_amdgcn_raw_buffer_load_b128(rof, lane * 16, (((h + (k >> 2)) & 3) * 36 + 32 + (k & 3)) * 1024, 16));
    wait2(aWOF, 8);
    {
      f32x4 a[4] = {};
      bf16x8 wf[2][8];
      auto rdw = [&](int i) {
        unsigned so = wo_slot(i) + e * 1024;
        asm volatile("" : "+s"(so));
#pragma unroll
        for (int k = 0; k < 8; ++k) {
          const unsigned ad = aT[k & 3] + so + (g >> 1) * 2048 + (k >> 2) * 8192;
          wf[i & 1][k] = cat(ldstr(ad), ldstr(ad + 4096));
        }
      };
      rdw(0);
#pragma unroll
      for (int i = 0; i < 8; ++i) {
        if (i < 7) rdw(i + 1);
        SB();
#pragma unroll
        for (int k = 0; k < 8; ++k) a[k & 3] = mfma32(wf[i & 1][k], fs[i * 2 + (k >> 2)], a[k & 3]);
        PIN(a[0]); PIN(a[1]); PIN(a[2]); PIN(a[3]);
        SB();
      }
      st128(__builtin_bit_cast(u32x4, pack8(a[0], a[1])), rp, lane * 16, (32 + 2 * e) * 1024, 16);
      st128(__builtin_bit_cast(u32x4, pack8(a[2], a[3])), rp, lane * 16, (33 + 2 * e) * 1024, 16);
      wait_vm<0>();
      if (lane == 0) set_flag(flag2 + 128 + e * 8 + (h >> 2));
    }
    STAMP(30);
    return;
  }

  const int w = wave, hh = lane >> 5, r32 = lane & 31;
  unsigned aV[2], aK[4];
  unsigned aMyDone = aDONE + w * 4, aF = aFULL;
  PIN(aMyDone); PIN(aF);
  {
    const unsigned q = i16 >> 2, p = i16 & 3, r8a = 4 * hh + q;
#pragma unroll
    for (int d = 0; d < 2; ++d) aV[d] = ldsb + r8a * 128 + (((4 * d + 2 * (g & 1) + (p >> 1)) ^ mv(r8a)) << 4) + 8 * (p & 1);
  }
  bf16x8 Q[8];
  {
    unsigned xo[4];
#pragma unroll
    for (int t = 0; t < 4; ++t) xo[t] = ldsb + 16384 + w * 4096 + (r32 >> 3) * 1024 + (r32 & 7) * 128 + (((2 * t + hh) ^ mv(r32 & 7) ^ ((r32 >> 3) & 1)) << 4);
    f32x16 acc[4] = {};
    bf16x8 fr[8], bo[2];
    auto rd = [&](auto rc, unsigned so) {
      constexpr int r = decltype(rc)::value;
      const unsigned a = aV[r & 1] + so + (r >> 2) * 4096 + ((r >> 1) & 1) * 1024;
      fr[r & 7] = cat(ldstr(a), ldstr(a + 2048));
    };
    wait2(aF, 1);
    STAMP(1);
    unsigned so = stg_off(0);
    bo[0] = lds128(xo[0] + so);
    rd(IntC<1>{}, so); rd(IntC<0>{}, so); rd(IntC<3>{}, so); rd(IntC<2>{}, so);
    for (int i = 0; i < 8; ++i) {
      unsigned sn = so;
      u32x2 fl = {0, 0};
      static_for<16>([&](auto pc) {
        constexpr int p = decltype(pc)::value;
        acc[p & 3] = mfmaL(fr[p & 7], bo[(p >> 2) & 1], acc[p & 3]); PIN(acc[p & 3]);
        SB();
        if constexpr (p == 12) {
          sn = so + STG; if (sn == STG0 + 3 * STG) sn = STG0;
          if (__builtin_expect((unsigned)__builtin_amdgcn_readfirstlane(min(fl[0], fl[1])) < (unsigned)(i + 2) && i < 7, 0)) {
            PIN(fr[5]); PIN(fr[6]); PIN(fr[7]);
            wait2(aF, i + 2);
          }
          MEMFENCE();
          STAMP(2 + i);
        }
        constexpr int r = ((p + 4) ^ 1) & 15;
        if constexpr ((p & 3) == 0) bo[((p + 4) >> 2) & 1] = lds128(xo[((p + 4) >> 2) & 3] + sn);
        rd(IntC<r>{}, sn);
        if constexpr (p == 8) fl = ldsw2(aF);
        if constexpr (p == 11) { MEMFENCE(); stsw(aMyDone, i + 1); }
        SB();
      });
      so = sn;
    }
    static_for<4>([&](auto dc) {
      constexpr int D = decltype(dc)::value;
      Q[2 * D] = pack8_contig32(quad<0>(acc[D]) * QSCALE, quad<1>(acc[D]) * QSCALE);
      Q[2 * D + 1] = pack8_contig32(quad<2>(acc[D]) * QSCALE, quad<3>(acc[D]) * QSCALE);
    });
  }
#pragma unroll
  for (int t = 0; t < 4; ++t) aK[t] = ldsb + (r32 >> 3) * 2048 + (r32 & 7) * 128 + (((2 * t + hh) ^ mv(r32 & 7) ^ ((r32 >> 3) & 1)) << 4);
  aV[0] += SLOT; aV[1] += SLOT;

  f32x16 O[4] = {}, nm = {}, Sa, Sb;
  float l0 = 0.f, l1 = 0.f;
  auto max16 = [&](const f32x16& S) {
    float a = max3f(S[0], S[1], S[2]), c = max3f(S[3], S[4], S[5]);
    a = max3f(a, S[6], S[7]); c = max3f(c, S[8], S[9]); a = max3f(a, S[10], S[11]); c = max3f(c, S[12], S[13]);
    return max2f(max3f(a, S[14], S[15]), c);
  };
  auto fixL = [&](f32x16& S, bool first) {
    float d = xmax32(max16(S));
    if (!first) d = fmaxf(d, 0.f);
    S -= d; nm -= d;
    if (!first) {
      const float al = ex2(-d);
#pragma unroll
      for (int D = 0; D < 4; ++D) O[D] *= al;
      l0 *= al; l1 *= al;
    }
  };

  bf16x8 fr[8];
  wait2(aF, 9);
  STAMP(10);
#pragma unroll
  for (int s = 0; s < 8; ++s) Sa = mfmaL(lds128(aK[s & 3] + (s >> 2) * 1024), Q[s], s ? Sa : nm);
  fr[1] = lds128(aK[1] + 8192); fr[0] = lds128(aK[0] + 8192); fr[3] = lds128(aK[3] + 8192); fr[2] = lds128(aK[2] + 8192);
  fixL(Sa, true);
  SB();

  int sk = 0, sv = 1;
  unsigned step = 10;
  auto body = [&](auto lastc) {
    constexpr bool LAST = decltype(lastc)::value;
    unsigned P[8];
    int mx = 0, my = 0;
    u32x2 fl = {0, 0};
    auto rd = [&](auto rc) {
      constexpr int r = decltype(rc)::value, rj = r & 7;
      if constexpr (r < 8) fr[rj] = lds128(aK[rj & 3] + 8192 + (rj >> 2) * 1024);
      else if constexpr (r < 16 || (r >= 24 && r < 32)) {
        const unsigned a = aV[rj & 1] + (r >= 24 ? 8192 : 0) + (rj >> 2) * 4096 + ((rj >> 1) & 1) * 1024;
        fr[rj] = cat(ldstr(a), ldstr(a + 2048));
      } else if constexpr (!LAST) fr[rj] = lds128(aK[rj & 3] + (r >= 32 ? 8192 : 0) + (rj >> 2) * 1024);
    };
    static_for<32>([&](auto pc) {
      constexpr int p = decltype(pc)::value, j = p & 7;
      if constexpr (p == 4 || (p == 12 && !LAST)) {
        if (__builtin_expect((unsigned)__builtin_amdgcn_readfirstlane(min(fl[0], fl[1])) < step, 0)) {
          PIN(fr[(p + 0) & 7]); PIN(fr[(p + 1) & 7]); PIN(fr[(p + 2) & 7]); PIN(fr[(p + 3) & 7]);
          wait2(aF, step);
        }
        MEMFENCE();
        if constexpr (p == 4) stsw(aMyDone, step - 1);
        ++step;
        if constexpr (p == 12) {
          const int dk = sk >= NSLOT - 2 ? (2 - NSLOT) * SLOT : 2 * SLOT;
          sk += sk >= NSLOT - 2 ? 2 - NSLOT : 2;
#pragma unroll
          for (int t = 0; t < 4; ++t) aK[t] += dk;
        }
      }
      if constexpr ((p & 1) == 0) {
        if constexpr (p == 0 || (p == 8 && !LAST)) fl = ldsw2(aF);
        rd(IntC<p + 5>{}); rd(IntC<p + 4>{});
      }
      if constexpr (p < 8 || (p >= 16 && p < 24)) {
        constexpr bool A = p < 8;
        f32x16& Sn = A ? Sb : Sa; f32x16& So = A ? Sa : Sb;
        if constexpr (A || !LAST) { Sn = mfmaL(fr[j], Q[j], j ? Sn : nm); PIN(Sn); }
        if constexpr ((j & 1) == 0) { So[2 * j] = ex2(So[2 * j]); }
        else {
          So[2 * j - 1] = ex2(So[2 * j - 1]); So[2 * j] = ex2(So[2 * j]); So[2 * j + 1] = ex2(So[2 * j + 1]);
          if constexpr (j > 1) { P[j - 3] = cvtpk(So[2 * j - 6], So[2 * j - 5]); P[j - 2] = cvtpk(So[2 * j - 4], So[2 * j - 3]); PIN(P[j - 3]); PIN(P[j - 2]); }
        }
        PIN(So);
      } else {
        constexpr bool A = p < 16;
        f32x16& Sn = A ? Sb : Sa; f32x16& So = A ? Sa : Sb;
        constexpr int t = j >> 2, D = j & 3;
        const u32x4 pw = {P[4 * t], P[4 * t + 1], P[4 * t + 2], P[4 * t + 3]};
        O[D] = mfmaL(fr[j], __builtin_bit_cast(bf16x8, pw), O[D]); PIN(O[D]);
        if constexpr (j == 0) { P[6] = cvtpk(So[12], So[13]); P[7] = cvtpk(So[14], So[15]); PIN(P[6]); PIN(P[7]); }
        if constexpr (j & 1) {
          l0 += So[2 * j - 2]; PIN(l0); l1 += So[2 * j - 1]; PIN(l1); l0 += So[2 * j]; PIN(l0); l1 += So[2 * j + 1]; PIN(l1);
          if constexpr (A || !LAST) {
            if constexpr (j == 1) { mx = imax3(Sn[0], Sn[1], Sn[2]); my = imax3(Sn[3], Sn[4], Sn[5]); }
            if constexpr (j == 3) { mx = imax3(mx, Sn[6], Sn[7]); my = imax3(my, Sn[8], Sn[9]); }
            if constexpr (j == 5) { mx = imax3(mx, Sn[10], Sn[11]); my = imax3(my, Sn[12], Sn[13]); }
            if constexpr (j == 7) { mx = imax3(mx, Sn[14], Sn[15]); mx = max(mx, my); }
            PIN(mx); PIN(my);
          }
        }
        if constexpr (j == 7 && (A || !LAST)) {
          SB();
          if (__builtin_expect(__any(mx > __float_as_int(THR)), 0)) {
            if constexpr (A && !LAST) { PIN(fr[0]); PIN(fr[1]); PIN(fr[2]); PIN(fr[3]); }
            fixL(Sn, false);
          }
        }
        if constexpr (p == 27) {
          const int dv = sv >= NSLOT - 2 ? (2 - NSLOT) * SLOT : 2 * SLOT;
          sv += sv >= NSLOT - 2 ? 2 - NSLOT : 2;
          aV[0] += dv; aV[1] += dv;
          if constexpr (!LAST) { MEMFENCE(); stsw(aMyDone, step - 2); }
        }
      }
      SB();
    });
  };
  for (int n = 0; n < 15; ++n) { STAMP(11 + n); body(BoolC<false>{}); }
  STAMP(26);
  body(BoolC<true>{});
  STAMP(27);

  int ln = __builtin_amdgcn_mbcnt_hi(~0u, __builtin_amdgcn_mbcnt_lo(~0u, 0u));
  PIN(ln);
  const unsigned l16 = ln * 16;
  bf16x8 fo[32];
  {
    const float rl = __builtin_amdgcn_rcpf(xadd32(l0 + l1));
    static_for<4>([&](auto dc) {
      constexpr int D = decltype(dc)::value;
      fo[2 * D] = pack8(quad<0>(O[D]) * rl, quad<1>(O[D]) * rl);
      fo[2 * D + 1] = pack8(quad<2>(O[D]) * rl, quad<3>(O[D]) * rl);
      st128(__builtin_bit_cast(u32x4, fo[2 * D]), rof, l16, ((h & 3) * 36 + w * 8 + 2 * D) * 1024, 16);
      st128(__builtin_bit_cast(u32x4, fo[2 * D + 1]), rof, l16, ((h & 3) * 36 + w * 8 + 2 * D + 1) * 1024, 16);
    });
  }
  {
    const unsigned g2 = ln >> 4, q = (ln & 15) >> 2, p = ln & 3, r8a = 4 * (ln >> 5) + q;
    const unsigned v0 = ldsb + r8a * 128 + (((2 * (g2 & 1) + (p >> 1)) ^ mv(r8a)) << 4) + 8 * (p & 1);
    const unsigned v1 = ldsb + r8a * 128 + (((4 + 2 * (g2 & 1) + (p >> 1)) ^ mv(r8a)) << 4) + 8 * (p & 1);
    f32x16 acc[4] = {};
    GAS unsigned* fl2 = flag2 + w * 32 + (h >> 2);
    unsigned av0 = 0, av1 = 0;
    auto rd = [&](auto rc) {
      constexpr int r = decltype(rc)::value;
      if constexpr (r < 128) {
        constexpr int i = r < 32 ? r >> 4 : 2 + ((r - 32) % 24) / 4;
        constexpr int t = r < 32 ? (r >> 2) & 3 : r & 3, D = r < 32 ? r & 3 : (r - 32) / 24;
        const unsigned a = ((D & 1) ? av1 : av0) + t * 4096 + (D >> 1) * 1024;
        fr[r & 7] = cat(ldstr(a), ldstr(a + 2048));
        (void)i;
      }
    };
    wait2(aWOF, 2);
    wait1(aHS, 1);
    static_for<132>([&](auto rc) {
      constexpr int r = decltype(rc)::value, pp = r - 4;
      if constexpr (pp == OWN_AT) {
        wait_vm<0>();
        if (ln == 0) set_flag(flag1 + (h & 3) * 32 + w);
        STAMP(28);
        wait_bits(aRDY, 15u << (4 * w));
#pragma unroll
        for (int k = 8; k < 32; ++k)
          fo[k] = __builtin_bit_cast(bf16x8, __builtin_amdgcn_raw_buffer_load_b128(rof, l16, (((h + (k >> 3)) & 3) * 36 + w * 8 + (k & 7)) * 1024, 16));
        STAMP(29);
      }
      if constexpr (pp >= 0) {
        constexpr int k = pp < 32 ? pp >> 2 : 8 + (pp - 32) % 24, D = pp < 32 ? pp & 3 : (pp - 32) / 24;
        acc[D] = mfmaL(fr[pp & 7], fo[k], acc[D]); PIN(acc[D]);
        SB();
      }
      if constexpr (r == 32) { wait2(aWOF, 8); STAMP(30); }
      if constexpr (r < 128) {
        if constexpr ((r < 32 && (r & 15) == 0) || (r >= 32 && (r & 3) == 0)) {
          constexpr int i = r < 32 ? r >> 4 : 2 + ((r - 32) % 24) / 4;
          unsigned so = wo_slot(i);
          asm volatile("" : "+s"(so));
          av0 = v0 + so; av1 = v1 + so;
        }
        rd(IntC<(r ^ 1)>{});
      }
      if constexpr (pp >= 0) {
        constexpr int D = pp < 32 ? pp & 3 : (pp - 32) / 24;
        if constexpr (pp >= 32 && (pp - 32) % 24 == 23) {
          SB();
          if constexpr (D >= 2) {
            wait_vm<(D == 2 ? 2 : 3)>();
            if (ln == 0) set_flag(fl2 + (D - 2) * 8);
          }
          st128(__builtin_bit_cast(u32x4, pack8(quad<0>(acc[D]), quad<1>(acc[D]))), rp, l16, (w * 8 + D * 2) * 1024, 16);
          st128(__builtin_bit_cast(u32x4, pack8(quad<2>(acc[D]), quad<3>(acc[D]))), rp, l16, (w * 8 + D * 2 + 1) * 1024, 16);
          stsw(aMyDone, 41 + D);
          STAMP(31 + D);
        }
      }
      SB();
    });
    wait_vm<3>();
    if (ln == 0) set_flag(fl2 + 2 * 8);
    wait_vm<1>();
    if (ln == 0) set_flag(fl2 + 3 * 8);
  }
  STAMP(35);
}

extern "C" void kernel_launch(void* const* d_in, const int* in_sizes, int n_in, void* d_out, int out_size, void* d_ws,
                              size_t ws_size, hipStream_t stream) {
  const uint16_t* x = (const uint16_t*)d_in[0];
  const uint16_t* kc = (const uint16_t*)d_in[1];
  const uint16_t* vc = (const uint16_t*)d_in[2];
  const uint16_t* wq = (const uint16_t*)d_in[3];
  const uint16_t* wo = (const uint16_t*)d_in[4];
  (void)hipFuncSetAttribute((const void*)attn_kernel, hipFuncAttributeMaxDynamicSharedMemorySize, LDS_BYTES);
  hipLaunchKernelGGL(attn_kernel, dim3(256), dim3(512), LDS_BYTES, stream, x, kc, vc, wq, wo, (char*)d_ws, (uint16_t*)d_out);
}
```

```cpp
#include <hip/hip_runtime.h>
#include <hip/hip_bf16.h>
#include <stdint.h>
#include <utility>

typedef __bf16 bf16x8 __attribute__((ext_vector_type(8)));
typedef __bf16 bf16x4 __attribute__((ext_vector_type(4)));
typedef __bf16 bf16x2 __attribute__((ext_vector_type(2)));
typedef short s16x4 __attribute__((ext_vector_type(4)));
typedef float f32x16 __attribute__((ext_vector_type(16)));
typedef float f32x4 __attribute__((ext_vector_type(4)));
typedef float f32x2 __attribute__((ext_vector_type(2)));
typedef unsigned u32x4 __attribute__((ext_vector_type(4)));
typedef unsigned u32x2 __attribute__((ext_vector_type(2)));
#define LAS __attribute__((address_space(3)))
#define GAS __attribute__((address_space(1)))
#define DI __device__ __forceinline__

constexpr int NB = 8, SQ = 144, DM = 512, NH = 32, SK = 1024, DH = 128;
constexpr int SLOT = 16384, NSLOT = 9, LDS_BYTES = 163840;
constexpr int STG0 = 3 * SLOT, STG = 34 * 1024, MISC_OFF = STG0 + 3 * STG;
constexpr int XCH_OFF = MISC_OFF;
constexpr int ML_OFF = MISC_OFF + 8192;
constexpr int CTL_OFF = MISC_OFF + 9216;
DI int stg_off(int i) { return STG0 + ((i + 1) % 3) * STG; }
#define KV_AUX 2
#define EARLY_MASK 0x54
#define WO_BYTES (NH * DH * DM * 2)
#define KV_DEPTH 2
#define S_HI 1
#define SPRIO(x) __builtin_amdgcn_s_setprio(x)
#define OWN_AT 28
#define WO_AUX 2
#define WX_AUX 0
constexpr size_t WS_FLAG_BYTES = 65536 + 8192, WS_OF = WS_FLAG_BYTES, WS_PART = WS_OF + (size_t)256 * 36 * 1024, WS_END = WS_PART + (size_t)256 * 36 * 1024;
constexpr int SPIN_CAP = 1 << 19;
constexpr float QSCALE = 0.088388347648318440f * 1.4426950408889634f;
#define THR_VAL 8.0f
constexpr float THR = THR_VAL;

DI f32x16 mfmaL(bf16x8 a, bf16x8 b, f32x16 c) { return __builtin_amdgcn_mfma_f32_32x32x16_bf16(a, b, c, 0, 0, 0); }
DI f32x4 mfma32(bf16x8 a, bf16x8 b, f32x4 c) { return __builtin_amdgcn_mfma_f32_16x16x32_bf16(a, b, c, 0, 0, 0); }
DI f32x4 mfma16(bf16x4 a, bf16x4 b, f32x4 c) {
  return __builtin_amdgcn_mfma_f32_16x16x16bf16_1k(__builtin_bit_cast(s16x4, a), __builtin_bit_cast(s16x4, b), c, 0, 0, 0);
}
DI unsigned cvtpk(float lo, float hi) { f32x2 v = {lo, hi}; bf16x2 b = __builtin_convertvector(v, bf16x2); return __builtin_bit_cast(unsigned, b); }
DI float max3f(float a, float b, float c) { return fmaxf(fmaxf(a, b), c); }
DI float max2f(float a, float b) { return fmaxf(a, b); }
DI int imax3(float a, float b, float c) { return max(max(__float_as_int(a), __float_as_int(b)), __float_as_int(c)); }
DI int imax3(int a, float b, float c) { return max(max(a, __float_as_int(b)), __float_as_int(c)); }
DI float ex2(float x) { return __builtin_amdgcn_exp2f(x); }
DI bf16x8 lds128(unsigned addr) { return __builtin_bit_cast(bf16x8, *(const volatile LAS u32x4*)(uintptr_t)addr); }
DI bf16x4 ldstr(unsigned addr) { return __builtin_amdgcn_ds_read_tr16_b64_v4bf16((LAS bf16x4*)(uintptr_t)addr); }
DI bf16x8 cat(bf16x4 a, bf16x4 b) { return __builtin_shufflevector(a, b, 0, 1, 2, 3, 4, 5, 6, 7); }
#define BAR() asm volatile("s_waitcnt lgkmcnt(0)\n\ts_barrier" ::: "memory")
#define RAWBAR() asm volatile("s_barrier" ::: "memory")
#define SB() __builtin_amdgcn_sched_barrier(0)
#define PIN(x) asm volatile("" : "+v"(x))
template <int I> struct IntC { static constexpr int value = I; };
template <bool V> struct BoolC { static constexpr bool value = V; };
template <class F, int... Is> DI void static_for_(F&& f, std::integer_sequence<int, Is...>) { (f(IntC<Is>{}), ...); }
template <int N, class F> DI void static_for(F&& f) { static_for_(f, std::make_integer_sequence<int, N>{}); }
DI float xmax32(float x) {
  auto b = __builtin_amdgcn_permlane32_swap(__float_as_uint(x), __float_as_uint(x), false, false);
  return max2f(__uint_as_float(b[0]), __uint_as_float(b[1]));
}
DI float xmax16(float x) {
  auto a = __builtin_amdgcn_permlane16_swap(__float_as_uint(x), __float_as_uint(x), false, false);
  return max2f(__uint_as_float(a[0]), __uint_as_float(a[1]));
}
DI float xadd32(float x) {
  auto b = __builtin_amdgcn_permlane32_swap(__float_as_uint(x), __float_as_uint(x), false, false);
  return __uint_as_float(b[0]) + __uint_as_float(b[1]);
}
DI float xadd16(float x) {
  auto b = __builtin_amdgcn_permlane16_swap(__float_as_uint(x), __float_as_uint(x), false, false);
  return __uint_as_float(b[0]) + __uint_as_float(b[1]);
}
DI bf16x8 pack8(f32x4 a, f32x4 b) {
  u32x4 w = {cvtpk(a[0], a[1]), cvtpk(a[2], a[3]), cvtpk(b[0], b[1]), cvtpk(b[2], b[3])};
  return __builtin_bit_cast(bf16x8, w);
}
template <int Q> DI f32x4 quad(const f32x16& v) { return f32x4{v[4 * Q], v[4 * Q + 1], v[4 * Q + 2], v[4 * Q + 3]}; }
DI bf16x8 pack8_contig16(f32x4 x, f32x4 y) {
  unsigned x0 = cvtpk(x[0], x[1]), x1 = cvtpk(x[2], x[3]), y0 = cvtpk(y[0], y[1]), y1 = cvtpk(y[2], y[3]);
  auto a0 = __builtin_amdgcn_permlane32_swap(x0, y0, false, false);
  auto a1 = __builtin_amdgcn_permlane32_swap(x1, y1, false, false);
  auto b0 = __builtin_amdgcn_permlane16_swap(a0[0], a0[1], false, false);
  auto b1 = __builtin_amdgcn_permlane16_swap(a1[0], a1[1], false, false);
  u32x4 w = {b0[0], b1[0], b0[1], b1[1]};
  return __builtin_bit_cast(bf16x8, w);
}
DI bf16x8 pack8_contig32(f32x4 x, f32x4 y) {
  unsigned x0 = cvtpk(x[0], x[1]), x1 = cvtpk(x[2], x[3]), y0 = cvtpk(y[0], y[1]), y1 = cvtpk(y[2], y[3]);
  auto a0 = __builtin_amdgcn_permlane32_swap(x0, y0, false, false);
  auto a1 = __builtin_amdgcn_permlane32_swap(x1, y1, false, false);
  u32x4 w = {a0[0], a1[0], a0[1], a1[1]};
  return __builtin_bit_cast(bf16x8, w);
}
template <int N> DI void wait_vm() { asm volatile("s_waitcnt vmcnt(%0)" ::"i"(N) : "memory"); }
DI void wait_flags(GAS unsigned* fp) {
#pragma unroll 1
  for (int it = 0; it < SPIN_CAP; ++it) {
    if (__all(__hip_atomic_load(fp, __ATOMIC_RELAXED, __HIP_MEMORY_SCOPE_AGENT) == 1u)) break;
    __builtin_amdgcn_s_sleep(1);
  }
}
DI __amdgpu_buffer_rsrc_t mk_rsrc(const void* p, unsigned bytes) { return __builtin_amdgcn_make_buffer_rsrc((void*)p, 0, bytes, 0x00020000); }
template <int AUX> DI void dma16(__amdgpu_buffer_rsrc_t r, unsigned ldsaddr, unsigned voff, unsigned soff) {
  __builtin_amdgcn_raw_ptr_buffer_load_lds(r, (LAS void*)(uintptr_t)ldsaddr, 16, voff, soff, 0, AUX);
}
DI void st128(u32x4 v, __amdgpu_buffer_rsrc_t r, unsigned voff, unsigned soff, int) {
  __builtin_amdgcn_raw_buffer_store_b128(v, r, voff, soff, 16);
  asm volatile("s_nop 1" ::"v"(v) : "memory");
}
DI unsigned mv(unsigned r8) { return ((r8 & 2) << 1) | ((r8 & 4) >> 1); }

template <class T> DI T sel(bool c, T a, T b) { return c ? a : b; }
DI unsigned ldsw(unsigned addr) { return *(const volatile LAS unsigned*)(uintptr_t)addr; }
DI u32x2 ldsw2(unsigned addr) { return *(const volatile LAS u32x2*)(uintptr_t)addr; }
DI u32x4 ldsw4(unsigned addr) { return *(const volatile LAS u32x4*)(uintptr_t)addr; }
DI void stsw(unsigned addr, unsigned v) { *(volatile LAS unsigned*)(uintptr_t)addr = v; }
#define MEMFENCE() asm volatile("" ::: "memory")
DI void pub(unsigned addr, unsigned v) {
  asm volatile("s_mov_b64 exec, 1\n\tds_max_u32 %0, %1\n\ts_mov_b64 exec, -1" ::"v"(addr), "v"(v) : "memory");
}
DI void wait2(unsigned addr, unsigned need) {
#pragma unroll 1
  for (int it = 0; it < SPIN_CAP; ++it) {
    const u32x2 v = ldsw2(addr);
    if ((unsigned)__builtin_amdgcn_readfirstlane(min(v[0], v[1])) >= need) break;
    __builtin_amdgcn_s_sleep(1);
  }
  MEMFENCE();
}
DI void wait1(unsigned addr, unsigned need) {
#pragma unroll 1
  for (int it = 0; it < SPIN_CAP; ++it) {
    if ((unsigned)__builtin_amdgcn_readfirstlane(ldsw(addr)) >= need) break;
    __builtin_amdgcn_s_sleep(1);
  }
  MEMFENCE();
}
DI void wait_bits(unsigned addr, unsigned bits) {
#pragma unroll 1
  for (int it = 0; it < SPIN_CAP; ++it) {
    if (((unsigned)__builtin_amdgcn_readfirstlane(ldsw(addr)) & bits) == bits) break;
    __builtin_amdgcn_s_sleep(1);
  }
  MEMFENCE();
}
DI unsigned done_min(unsigned addr) {
  u32x4 a; u32x2 c;
  asm volatile("ds_read_b128 %0, %2\n\tds_read_b64 %1, %2 offset:16\n\ts_waitcnt lgkmcnt(0)" : "=&v"(a), "=&v"(c) : "v"(addr) : "memory");
  return __builtin_amdgcn_readfirstlane(min(min(min(a[0], a[1]), min(a[2], a[3])), min(c[0], c[1])));
}
DI void set_flag(GAS unsigned* p) { __hip_atomic_store(p, 1u, __ATOMIC_RELAXED, __HIP_MEMORY_SCOPE_AGENT); }
DI unsigned wo_slot(int i) { return ((32 + i) % NSLOT) * SLOT; }

DI f32x4 bf_lo(u32x4 v) { return f32x4{__uint_as_float(v[0] << 16), __uint_as_float(v[0] & 0xffff0000u), __uint_as_float(v[1] << 16), __uint_as_float(v[1] & 0xffff0000u)}; }
DI f32x4 bf_hi(u32x4 v) { return f32x4{__uint_as_float(v[2] << 16), __uint_as_float(v[2] & 0xffff0000u), __uint_as_float(v[3] << 16), __uint_as_float(v[3] & 0xffff0000u)}; }
DI void reduce_blocks(char* ws, uint16_t* out, int b, int h, int L, int lane) {
  const int hg = h >> 2, nt = h & 3;
  const __amdgpu_buffer_rsrc_t rp = mk_rsrc(ws + WS_PART + (size_t)(b * 4 + nt) * 8 * 36 * 1024, 8 * 36 * 1024);
  GAS unsigned* flag2 = (GAS unsigned*)(ws + 32768) + (b * 4 + nt) * 256;
  __builtin_amdgcn_s_setprio(0);
  auto sum8 = [&](const u32x4(&v)[8], f32x4& x, f32x4& y) {
    x = ((bf_lo(v[0]) + bf_lo(v[1])) + (bf_lo(v[2]) + bf_lo(v[3]))) + ((bf_lo(v[4]) + bf_lo(v[5])) + (bf_lo(v[6]) + bf_lo(v[7])));
    y = ((bf_hi(v[0]) + bf_hi(v[1])) + (bf_hi(v[2]) + bf_hi(v[3]))) + ((bf_hi(v[4]) + bf_hi(v[5])) + (bf_hi(v[6]) + bf_hi(v[7])));
  };
  {
    wait_flags(flag2 + ((2 * L + ((lane >> 3) & 1)) * 4 + (hg >> 1)) * 8 + (lane & 7));
    __builtin_amdgcn_fence(__ATOMIC_ACQUIRE, "wavefront");
    u32x4 v0[8], v1[8];
#pragma unroll
    for (int u = 0; u < 8; ++u) {
      v0[u] = __builtin_amdgcn_raw_buffer_load_b128(rp, lane * 16, (u * 36 + 16 * L + hg) * 1024, 16);
      v1[u] = __builtin_amdgcn_raw_buffer_load_b128(rp, lane * 16, (u * 36 + 16 * L + 8 + hg) * 1024, 16);
    }
    f32x4 x, y;
    uint16_t* o = out + ((size_t)(b * SQ + L * 64 + (lane & 31)) * DM + nt * 128 + hg * 16 + (lane >> 5) * 8);
    sum8(v0, x, y); *(u32x4*)o = __builtin_bit_cast(u32x4, pack8_contig32(x, y));
    sum8(v1, x, y); *(u32x4*)(o + 32 * DM) = __builtin_bit_cast(u32x4, pack8_contig32(x, y));
  }
  if (hg < 2) {
    const int wp = 2 * hg + L;
    wait_flags(flag2 + 128 + (wp >> 1) * 8 + (lane & 7));
    __builtin_amdgcn_fence(__ATOMIC_ACQUIRE, "wavefront");
    u32x4 v[8];
#pragma unroll
    for (int u = 0; u < 8; ++u) v[u] = __builtin_amdgcn_raw_buffer_load_b128(rp, lane * 16, (u * 36 + 32 + wp) * 1024, 16);
    f32x4 x, y; sum8(v, x, y);
    *(u32x4*)(out + ((size_t)(b * SQ + 128 + (lane & 15)) * DM + nt * 128 + wp * 32 + (lane >> 4) * 8)) = __builtin_bit_cast(u32x4, pack8_contig16(x, y));
  }
}

struct Handshake {
  GAS unsigned* base;
  GAS unsigned* mine;
  unsigned v, lastL, N; int phase; bool done, first, leader; int h;
  DI void init(char* ws, int b, int h_, int lane, GAS unsigned* flag1, GAS unsigned* flag2) {
    h = h_; base = (GAS unsigned*)(ws + 65536) + b * 256;
    GAS unsigned* c = lane < 6 ? flag1 + (h & 3) * 32 + lane
                    : lane < 22 ? flag2 + (lane - 6) * 8 + (h >> 2)
                    : lane < 24 ? flag2 + 128 + (lane - 22) * 8 + (h >> 2) : base + 64 + h;
    if (lane < 25) __hip_atomic_store(c, 0u, __ATOMIC_RELAXED, __HIP_MEMORY_SCOPE_AGENT);
    wait_vm<0>();
    mine = lane < 32 ? base + lane : (lane == 32 ? base + 32 : base + 64 + h);
    leader = h == 0; phase = 0; done = false; first = true; lastL = 0; N = 0;
    v = __hip_atomic_load(mine, __ATOMIC_RELAXED, __HIP_MEMORY_SCOPE_AGENT);
  }
  DI void step(int lane, unsigned aHS) {
    if (done) return;
    const unsigned vL = __builtin_amdgcn_readlane(v, 32), vI = __builtin_amdgcn_readlane(v, 33);
    if (!first && vI == 1u) { done = true; stsw(aHS, 1); return; }
    if (leader) {
      if (phase == 0) {
        N = vL + 1;
        for (int t = 0; t < 34 && __any(lane < 32 && v == N); ++t) { ++N; if (N == vL) ++N; }
        if (lane == 0) __hip_atomic_store(base + 32, N, __ATOMIC_RELAXED, __HIP_MEMORY_SCOPE_AGENT);
        phase = 1;
      } else if (phase == 1 && __all(lane >= 32 || v == N)) {
        if (lane < 32) __hip_atomic_store(base + 64 + lane, 1u, __ATOMIC_RELAXED, __HIP_MEMORY_SCOPE_AGENT);
        phase = 2;
      }
    }
    if (first || vL != lastL) {
      if (lane == 0) __hip_atomic_store(base + h, vL, __ATOMIC_RELAXED, __HIP_MEMORY_SCOPE_AGENT);
      lastL = vL; first = false;
    }
    v = __hip_atomic_load(mine, __ATOMIC_RELAXED, __HIP_MEMORY_SCOPE_AGENT);
  }
};

__global__ __launch_bounds__(512, 2) void attn_kernel(const uint16_t* __restrict__ x, const uint16_t* __restrict__ kc,
                                                      const uint16_t* __restrict__ vc, const uint16_t* __restrict__ wq,
                                                      const uint16_t* __restrict__ wo, char* __restrict__ ws,
                                                      uint16_t* __restrict__ out) {
  extern __shared__ __attribute__((aligned(16))) char lds[];
  const int tid = threadIdx.x, lane = tid & 63;
  const int wave = __builtin_amdgcn_readfirstlane(tid >> 6);
  const int bid = blockIdx.x;
  const int h = 4 * (bid & 7) + ((bid >> 3) & 3), b = bid >> 5;
  const unsigned ldsb = (unsigned)(uintptr_t)(LAS char*)lds;
  const unsigned aFULL = ldsb + CTL_OFF, aWOF = aFULL + 8, aDONE = aFULL + 16, aPAIR = aFULL + 48, aHS = aFULL + 56, aRDY = aFULL + 60;
  const __amdgpu_buffer_rsrc_t rof = mk_rsrc(ws + WS_OF + (size_t)(b * NH + (h & ~3)) * 36 * 1024, 4 * 36 * 1024);
  GAS unsigned* flag1 = (GAS unsigned*)ws + (b * NH + (h & ~3)) * 32;
  GAS unsigned* flag2 = (GAS unsigned*)(ws + 32768) + (b * 4 + (h & 3)) * 256;
  const __amdgpu_buffer_rsrc_t rp = mk_rsrc(ws + WS_PART + (size_t)((b * 4 + (h & 3)) * 8 + (h >> 2)) * 36 * 1024, 36 * 1024);

  if (wave >= 6) {
    __builtin_amdgcn_s_setprio(3);
    const unsigned half = wave - 6, r8 = lane >> 3, sw = ((lane & 7) ^ mv(r8)) << 4;
    const unsigned aMy = aFULL + half * 4, aMyW = aWOF + half * 4;
    const size_t hb = (size_t)(b * NH + h) * SK * DH;
    const __amdgpu_buffer_rsrc_t rk = mk_rsrc(kc + hb, SK * DH * 2), rv = mk_rsrc(vc + hb, SK * DH * 2), rw = mk_rsrc(wo, WO_BYTES);
    const unsigned voff = r8 * 256u + sw, voffx = voff ^ 16u, voffw = r8 * 1024u + sw;
    const unsigned aPoll = aDONE + (lane < 6 ? lane : 0) * 4;
    auto busy = [&](unsigned need) {
      unsigned v;
      asm volatile("ds_read_b32 %0, %1\n\ts_waitcnt lgkmcnt(0)" : "=v"(v) : "v"(aPoll) : "memory");
      return __any(v < need);
    };
    const unsigned dsth = ldsb + half * 8192;
    unsigned src = half * 8192u;
    auto dmaK = [&](unsigned dst) {
#pragma unroll
      for (int m = 0; m < 8; ++m) dma16<KV_AUX>(rk, dst + m * 1024, (m & 2) ? voffx : voff, src + (m >> 1) * 2048 + (m & 1) * 128);
    };
    auto dmaV = [&](unsigned dst) {
#pragma unroll
      for (int m = 0; m < 8; ++m) dma16<KV_AUX>(rv, dst + m * 1024, voff, src + (m >> 1) * 2048 + (m & 1) * 128);
      src += 16384u;
    };
    auto dmaW = [&](unsigned dst, int i) {
      const unsigned sw_ = (unsigned)((h >> 2) * 512 + ((h + (i >> 1)) & 3) * 128 + (i & 1) * 64 + half * 32) * 1024u + (h & 3) * 256u;
#pragma unroll
      for (int m = 0; m < 8; ++m) dma16<WO_AUX>(rw, dst + m * 1024, voffw, sw_ + (m >> 1) * 8192 + (m & 1) * 128);
    };
    {
      const bool isw = half == 0;
      const __amdgpu_buffer_rsrc_t rs = isw ? mk_rsrc(wq, DM * NH * DH * 2) : mk_rsrc(x + (size_t)b * SQ * DM, SQ * DM * 2);
      const unsigned voff0 = isw ? ((r8 & 3) + 8 * (r8 >> 2)) * 8192u + sw : r8 * 1024u + sw;
      const unsigned voff1 = isw ? voff0 + 128u : (voff0 ^ 16u) + 8192u;
      const int rot = b + 2 * (h & 3);
      const unsigned pstep0 = isw ? 4u * 8192u : 16384u, pstep1 = isw ? 12u * 8192u : 16384u;
      auto issue = [&](int st) {
        unsigned dst = ldsb + stg_off(st) + (isw ? 0 : 16384);
        const int i = (st + rot) & 7;
        unsigned so = isw ? (unsigned)i * 64u * 8192u + (unsigned)h * 256u : (unsigned)i * 128u;
#pragma unroll
        for (int m = 0; m < 8; ++m) {
          dma16<WX_AUX>(rs, dst, voff0, so); dma16<WX_AUX>(rs, dst + 1024, voff1, so);
          dst += 2048; so += (m & 1) ? pstep1 : pstep0;
        }
        if (!isw) { dma16<WX_AUX>(rs, dst, voff0, so); dma16<WX_AUX>(rs, dst + 1024, voff1, so); }
      };
      issue(0);
      BAR();
      bool kvb = false;
      int je = 0;
#pragma unroll 1
      for (int st = 1; st < 8; ++st) {
        if (st >= 3 && busy(st - 2)) {
          if (kvb) wait_vm<8>(); else wait_vm<0>();
          pub(aMy, st);
#pragma unroll 1
          for (int it = 0; it < SPIN_CAP && busy(st - 2); ++it) __builtin_amdgcn_s_sleep(1);
        }
        issue(st);
        if (kvb) { if (isw) wait_vm<24>(); else wait_vm<26>(); }
        else { if (isw) wait_vm<16>(); else wait_vm<18>(); }
        pub(aMy, st);
        kvb = (EARLY_MASK >> st) & 1;
        if (kvb) { if (je == 1) dmaV(dsth + SLOT); else dmaK(dsth + je * SLOT); ++je; }
      }
    }
    unsigned full = 11;
    auto acquire = [&](unsigned need) {
      if (busy(need)) {
        wait_vm<16>(); pub(aMy, full - 2);
        if (busy(need)) {
          wait_vm<8>(); pub(aMy, full - 1);
          if (busy(need)) {
            wait_vm<0>(); pub(aMy, full);
#pragma unroll 1
            for (int it = 0; it < SPIN_CAP && busy(need); ++it) __builtin_amdgcn_s_sleep(1);
          }
        }
      }
    };
    if (busy(6)) {
      wait_vm<0>(); pub(aMy, 11);
#pragma unroll 1
      for (int it = 0; it < SPIN_CAP && busy(6); ++it) __builtin_amdgcn_s_sleep(1);
    }
    dmaV(dsth + 3 * SLOT); wait_vm<8>(); pub(aMy, 11); full = 12;
    dmaK(dsth + 4 * SLOT); full = 13;
    acquire(7); dmaV(dsth + 5 * SLOT); full = 14;
    dmaK(dsth + 6 * SLOT); full = 15; wait_vm<24>(); pub(aMy, 12);
    acquire(8); dmaV(dsth + 7 * SLOT); full = 16; wait_vm<24>(); pub(aMy, 13);
    unsigned dst = dsth + 8 * SLOT;
    const unsigned dend = dsth + NSLOT * SLOT;
#pragma unroll 1
    for (int pr = 4; pr < 16; ++pr) {
      acquire(full - 8); dmaK(dst); ++full; dst += SLOT; if (dst == dend) dst = dsth;
      wait_vm<8 * KV_DEPTH>(); pub(aMy, full - KV_DEPTH);
      acquire(full - 8); dmaV(dst); ++full; dst += SLOT; if (dst == dend) dst = dsth;
      wait_vm<8 * KV_DEPTH>(); pub(aMy, full - KV_DEPTH);
    }
    wait_vm<16>(); pub(aMy, 38);
#pragma unroll 1
    for (int i = 0; i < 8; ++i) {
      if (busy(32 + i)) {
        wait_vm<8>(); pub(aMy, i == 0 ? 39 : 40);
        wait_vm<0>(); pub(aMy, 40); pub(aMyW, i);
#pragma unroll 1
        for (int it = 0; it < SPIN_CAP && busy(32 + i); ++it) __builtin_amdgcn_s_sleep(1);
      }
      dmaW(dst, i); dst += SLOT; if (dst == dend) dst = dsth;
      wait_vm<16>(); pub(aMy, i < 1 ? 39 : 40); if (i >= 2) pub(aMyW, i - 1);
    }
    wait_vm<0>(); pub(aMy, 40); pub(aMyW, 8);
    wait1(aHS, 1);
    if (half == 0) {
      GAS unsigned* fp = flag1 + (lane & 3) * 32 + (lane < 24 ? lane >> 2 : 0);
#pragma unroll 1
      for (int it = 0; it < SPIN_CAP; ++it) {
        const unsigned m = (unsigned)__ballot(__hip_atomic_load(fp, __ATOMIC_RELAXED, __HIP_MEMORY_SCOPE_AGENT) == 1u) & 0xFFFFFFu;
        stsw(aRDY, m);
        if (m == 0xFFFFFFu) break;
      }
    }
    {
      const unsigned a4 = aDONE + (lane & 3) * 4, need = 41 + (h >> 3);
#pragma unroll 1
      for (int it = 0; it < SPIN_CAP; ++it) {
        if (__all(ldsw(a4) >= need)) break;
        __builtin_amdgcn_s_sleep(2);
      }
    }
    reduce_blocks(ws, out, b, h, wave - 6, lane);
    return;
  }

  if (wave == 0 && lane < 16) stsw(aFULL + lane * 4, 0);
  BAR();
  const int g = lane >> 4, i16 = lane & 15;

  if (wave >= 4) {
    const int e = wave - 4;
    const unsigned aMyDone = aDONE + wave * 4;
    const unsigned q = i16 >> 2, p = i16 & 3, r8b = 4 * (g & 1) + q;
    unsigned aT[4];
#pragma unroll
    for (int t = 0; t < 4; ++t) aT[t] = ldsb + r8b * 128 + (((2 * t + (p >> 1)) ^ mv(r8b)) << 4) + 8 * (p & 1);
    unsigned aR[2];
#pragma unroll
    for (int t = 0; t < 2; ++t) aR[t] = (i16 & 7) * 128 + (((4 * t + g) ^ mv(i16 & 7) ^ (i16 >> 3)) << 4);
    Handshake hs;
    hs.done = true;
    if (e == 0) hs.init(ws, b, h, lane, flag1, flag2);
    bf16x8 Q[4];
    {
      f32x4 a[4] = {};
      for (int i = 0; i < 8; ++i) {
        wait2(aFULL, i + 1);
        const unsigned so = stg_off(i);
        bf16x8 xb[2], wf[8];
#pragma unroll
        for (int kk = 0; kk < 2; ++kk) {
          xb[kk] = lds128(ldsb + so + 32768 + (i16 >> 3) * 1024 + aR[kk]);
#pragma unroll
          for (int t = 0; t < 4; ++t) {
            const unsigned ad = aT[t] + so + e * 1024 + (g >> 1) * 4096 + kk * 8192;
            wf[kk * 4 + t] = cat(ldstr(ad), ldstr(ad + 2048));
          }
        }
        MEMFENCE();
        stsw(aMyDone, i + 1);
        SB();
#pragma unroll
        for (int k = 0; k < 8; ++k) a[k & 3] = mfma32(wf[k], xb[k >> 2], a[k & 3]);
      }
      *(LAS bf16x8*)(uintptr_t)(ldsb + XCH_OFF + (2 * e) * 1024 + lane * 16) = pack8_contig16(a[0] * QSCALE, a[1] * QSCALE);
      *(LAS bf16x8*)(uintptr_t)(ldsb + XCH_OFF + (2 * e + 1) * 1024 + lane * 16) = pack8_contig16(a[2] * QSCALE, a[3] * QSCALE);
      MEMFENCE();
      stsw(aPAIR + e * 4, 1);
      wait2(aPAIR, 1);
#pragma unroll
      for (int s = 0; s < 4; ++s) Q[s] = lds128(ldsb + XCH_OFF + s * 1024 + lane * 16);
    }
    f32x4 Os[8] = {}; float nm = 0.f, ls = 0.f;
    int slot = 0;
    for (int n = 0; n < 16; ++n) {
      hs.step(lane, aHS);
      SPRIO(0);
      wait2(aFULL, 9 + 2 * n);
      SPRIO(S_HI);
      const unsigned sk = ldsb + slot * SLOT + e * 8192 + (i16 >> 3) * 2048;
      f32x4 S0 = {nm, nm, nm, nm}, S1 = S0;
      bf16x8 kf[8];
#pragma unroll
      for (int s = 0; s < 4; ++s) { kf[s] = lds128(sk + aR[s & 1] + (s >> 1) * 1024); kf[4 + s] = lds128(sk + 4096 + aR[s & 1] + (s >> 1) * 1024); }
      MEMFENCE();
      stsw(aMyDone, 9 + 2 * n);
      SB();
#pragma unroll
      for (int s = 0; s < 4; ++s) { S0 = mfma32(kf[s], Q[s], S0); S1 = mfma32(kf[4 + s], Q[s], S1); }
      PIN(S0); PIN(S1);
      SPRIO(0);
      wait2(aFULL, 10 + 2 * n);
      SPRIO(S_HI);
      slot = slot == NSLOT - 1 ? 0 : slot + 1;
      const unsigned sv = slot * SLOT + e * 8192 + (g >> 1) * 2048;
#pragma unroll
      for (int T = 0; T < 8; ++T) {
        const unsigned ad = aT[T & 3] + sv + (T >> 2) * 1024;
        kf[T] = cat(ldstr(ad), ldstr(ad + 4096));
      }
      MEMFENCE();
      stsw(aMyDone, 10 + 2 * n);
      SB();
      const float mx = max2f(max2f(max2f(S0[0], S0[1]), max2f(S0[2], S0[3])), max2f(max2f(S1[0], S1[1]), max2f(S1[2], S1[3])));
      if (n == 0 || __any(mx > THR)) {
        float d = xmax32(xmax16(mx));
        if (n) d = fmaxf(d, 0.f);
        S0 -= d; S1 -= d; nm -= d;
        const float al = n ? ex2(-d) : 0.f;
#pragma unroll
        for (int T = 0; T < 8; ++T) Os[T] *= al;
        ls *= al;
      }
#pragma unroll
      for (int r = 0; r < 4; ++r) { S0[r] = ex2(S0[r]); S1[r] = ex2(S1[r]); }
      ls += ((S0[0] + S0[1]) + (S0[2] + S0[3])) + ((S1[0] + S1[1]) + (S1[2] + S1[3]));
      const bf16x8 P = pack8(S0, S1);
#pragma unroll
      for (int T = 0; T < 8; ++T) Os[T] = mfma32(kf[T], P, Os[T]);
      slot = slot == NSLOT - 1 ? 0 : slot + 1;
    }
#pragma unroll 1
    for (int it = 0; it < SPIN_CAP && !hs.done; ++it) hs.step(lane, aHS);
    wait1(aHS, 1);
    bf16x8 fs[16];
    {
      ls = xadd32(xadd16(ls));
#pragma unroll
      for (int t = 0; t < 4; ++t) *(LAS f32x4*)(uintptr_t)(ldsb + XCH_OFF + (e * 4 + t) * 1024 + lane * 16) = e ? Os[t] : Os[4 + t];
      *(LAS f32x2*)(uintptr_t)(ldsb + ML_OFF + e * 512 + lane * 8) = f32x2{-nm, ls};
      MEMFENCE();
      stsw(aPAIR + e * 4, 2);
      wait2(aPAIR, 2);
      const f32x2 ml = *(const LAS f32x2*)(uintptr_t)(ldsb + ML_OFF + (1 - e) * 512 + lane * 8);
      const float M = max2f(-nm, ml[0]);
      const float f0 = ex2(-nm - M), f1 = ex2(ml[0] - M);
      const float rL = __builtin_amdgcn_rcpf(f0 * ls + f1 * ml[1]);
      f32x4 o[4];
#pragma unroll
      for (int t = 0; t < 4; ++t)
        o[t] = (e ? Os[4 + t] : Os[t]) * (f0 * rL) + *(const LAS f32x4*)(uintptr_t)(ldsb + XCH_OFF + ((1 - e) * 4 + t) * 1024 + lane * 16) * (f1 * rL);
      const bf16x8 o0 = pack8(o[0], o[1]), o1 = pack8(o[2], o[3]);
      st128(__builtin_bit_cast(u32x4, o0), rof, lane * 16, ((h & 3) * 36 + 32 + 2 * e) * 1024, 16);
      st128(__builtin_bit_cast(u32x4, o1), rof, lane * 16, ((h & 3) * 36 + 33 + 2 * e) * 1024, 16);
      wait_vm<0>();
      if (lane == 0) set_flag(flag1 + (h & 3) * 32 + 4 + e);
    }
    wait_bits(aRDY, 0xFF0000u);
#pragma unroll
    for (int k = 0; k < 16; ++k)
      fs[k] = __builtin_bit_cast(bf16x8, __builtin_amdgcn_raw_buffer_load_b128(rof, lane * 16, (((h + (k >> 2)) & 3) * 36 + 32 + (k & 3)) * 1024, 16));
    wait2(aWOF, 8);
    {
      f32x4 a[4] = {};
      bf16x8 wf[2][8];
      auto rdw = [&](int i) {
        unsigned so = wo_slot(i) + e * 1024;
        asm volatile("" : "+s"(so));
#pragma unroll
        for (int k = 0; k < 8; ++k) {
          const unsigned ad = aT[k & 3] + so + (g >> 1) * 2048 + (k >> 2) * 8192;
          wf[i & 1][k] = cat(ldstr(ad), ldstr(ad + 4096));
        }
      };
      rdw(0);
#pragma unroll
      for (int i = 0; i < 8; ++i) {
        if (i < 7) rdw(i + 1);
        SB();
#pragma unroll
        for (int k = 0; k < 8; ++k) a[k & 3] = mfma32(wf[i & 1][k], fs[i * 2 + (k >> 2)], a[k & 3]);
        PIN(a[0]); PIN(a[1]); PIN(a[2]); PIN(a[3]);
        SB();
      }
      st128(__builtin_bit_cast(u32x4, pack8(a[0], a[1])), rp, lane * 16, (32 + 2 * e) * 1024, 16);
      st128(__builtin_bit_cast(u32x4, pack8(a[2], a[3])), rp, lane * 16, (33 + 2 * e) * 1024, 16);
      wait_vm<0>();
      if (lane == 0) set_flag(flag2 + 128 + e * 8 + (h >> 2));
    }
    return;
  }

  const int w = wave, hh = lane >> 5, r32 = lane & 31;
  unsigned aV[2], aK[4];
  unsigned aMyDone = aDONE + w * 4, aF = aFULL;
  PIN(aMyDone); PIN(aF);
  {
    const unsigned q = i16 >> 2, p = i16 & 3, r8a = 4 * hh + q;
#pragma unroll
    for (int d = 0; d < 2; ++d) aV[d] = ldsb + r8a * 128 + (((4 * d + 2 * (g & 1) + (p >> 1)) ^ mv(r8a)) << 4) + 8 * (p & 1);
  }
  bf16x8 Q[8];
  {
    unsigned xo[4];
#pragma unroll
    for (int t = 0; t < 4; ++t) xo[t] = ldsb + 16384 + w * 4096 + (r32 >> 3) * 1024 + (r32 & 7) * 128 + (((2 * t + hh) ^ mv(r32 & 7) ^ ((r32 >> 3) & 1)) << 4);
    f32x16 acc[4] = {};
    bf16x8 fr[8], bo[2];
    auto rd = [&](auto rc, unsigned so) {
      constexpr int r = decltype(rc)::value;
      const unsigned a = aV[r & 1] + so + (r >> 2) * 4096 + ((r >> 1) & 1) * 1024;
      fr[r & 7] = cat(ldstr(a), ldstr(a + 2048));
    };
    wait2(aF, 1);
    unsigned so = stg_off(0);
    bo[0] = lds128(xo[0] + so);
    rd(IntC<1>{}, so); rd(IntC<0>{}, so); rd(IntC<3>{}, so); rd(IntC<2>{}, so);
    for (int i = 0; i < 8; ++i) {
      unsigned sn = so;
      u32x2 fl = {0, 0};
      static_for<16>([&](auto pc) {
        constexpr int p = decltype(pc)::value;
        acc[p & 3] = mfmaL(fr[p & 7], bo[(p >> 2) & 1], acc[p & 3]); PIN(acc[p & 3]);
        SB();
        if constexpr (p == 12) {
          sn = so + STG; if (sn == STG0 + 3 * STG) sn = STG0;
          if (__builtin_expect((unsigned)__builtin_amdgcn_readfirstlane(min(fl[0], fl[1])) < (unsigned)(i + 2) && i < 7, 0)) {
            PIN(fr[5]); PIN(fr[6]); PIN(fr[7]);
            wait2(aF, i + 2);
          }
          MEMFENCE();
        }
        constexpr int r = ((p + 4) ^ 1) & 15;
        if constexpr ((p & 3) == 0) bo[((p + 4) >> 2) & 1] = lds128(xo[((p + 4) >> 2) & 3] + sn);
        rd(IntC<r>{}, sn);
        if constexpr (p == 8) fl = ldsw2(aF);
        if constexpr (p == 11) { MEMFENCE(); stsw(aMyDone, i + 1); }
        SB();
      });
      so = sn;
    }
    static_for<4>([&](auto dc) {
      constexpr int D = decltype(dc)::value;
      Q[2 * D] = pack8_contig32(quad<0>(acc[D]) * QSCALE, quad<1>(acc[D]) * QSCALE);
      Q[2 * D + 1] = pack8_contig32(quad<2>(acc[D]) * QSCALE, quad<3>(acc[D]) * QSCALE);
    });
  }
#pragma unroll
  for (int t = 0; t < 4; ++t) aK[t] = ldsb + (r32 >> 3) * 2048 + (r32 & 7) * 128 + (((2 * t + hh) ^ mv(r32 & 7) ^ ((r32 >> 3) & 1)) << 4);
  aV[0] += SLOT; aV[1] += SLOT;

  f32x16 O[4] = {}, nm = {}, Sa, Sb;
  float l0 = 0.f, l1 = 0.f;
  auto max16 = [&](const f32x16& S) {
    float a = max3f(S[0], S[1], S[2]), c = max3f(S[3], S[4], S[5]);
    a = max3f(a, S[6], S[7]); c = max3f(c, S[8], S[9]); a = max3f(a, S[10], S[11]); c = max3f(c, S[12], S[13]);
    return max2f(max3f(a, S[14], S[15]), c);
  };
  auto fixL = [&](f32x16& S, bool first) {
    float d = xmax32(max16(S));
    if (!first) d = fmaxf(d, 0.f);
    S -= d; nm -= d;
    if (!first) {
      const float al = ex2(-d);
#pragma unroll
      for (int D = 0; D < 4; ++D) O[D] *= al;
      l0 *= al; l1 *= al;
    }
  };

  bf16x8 fr[8];
  wait2(aF, 9);
#pragma unroll
  for (int s = 0; s < 8; ++s) Sa = mfmaL(lds128(aK[s & 3] + (s >> 2) * 1024), Q[s], s ? Sa : nm);
  fr[1] = lds128(aK[1] + 8192); fr[0] = lds128(aK[0] + 8192); fr[3] = lds128(aK[3] + 8192); fr[2] = lds128(aK[2] + 8192);
  fixL(Sa, true);
  SB();

  int sk = 0, sv = 1;
  unsigned step = 10;
  auto body = [&](auto lastc) {
    constexpr bool LAST = decltype(lastc)::value;
    unsigned P[8];
    int mx = 0, my = 0;
    u32x2 fl = {0, 0};
    auto rd = [&](auto rc) {
      constexpr int r = decltype(rc)::value, rj = r & 7;
      if constexpr (r < 8) fr[rj] = lds128(aK[rj & 3] + 8192 + (rj >> 2) * 1024);
      else if constexpr (r < 16 || (r >= 24 && r < 32)) {
        const unsigned a = aV[rj & 1] + (r >= 24 ? 8192 : 0) + (rj >> 2) * 4096 + ((rj >> 1) & 1) * 1024;
        fr[rj] = cat(ldstr(a), ldstr(a + 2048));
      } else if constexpr (!LAST) fr[rj] = lds128(aK[rj & 3] + (r >= 32 ? 8192 : 0) + (rj >> 2) * 1024);
    };
    static_for<32>([&](auto pc) {
      constexpr int p = decltype(pc)::value, j = p & 7;
      if constexpr (p == 4 || (p == 12 && !LAST)) {
        if (__builtin_expect((unsigned)__builtin_amdgcn_readfirstlane(min(fl[0], fl[1])) < step, 0)) {
          PIN(fr[(p + 0) & 7]); PIN(fr[(p + 1) & 7]); PIN(fr[(p + 2) & 7]); PIN(fr[(p + 3) & 7]);
          wait2(aF, step);
        }
        MEMFENCE();
        if constexpr (p == 4) stsw(aMyDone, step - 1);
        ++step;
        if constexpr (p == 12) {
          const int dk = sk >= NSLOT - 2 ? (2 - NSLOT) * SLOT : 2 * SLOT;
          sk += sk >= NSLOT - 2 ? 2 - NSLOT : 2;
#pragma unroll
          for (int t = 0; t < 4; ++t) aK[t] += dk;
        }
      }
      if constexpr ((p & 1) == 0) {
        if constexpr (p == 0 || (p == 8 && !LAST)) fl = ldsw2(aF);
        rd(IntC<p + 5>{}); rd(IntC<p + 4>{});
      }
      if constexpr (p < 8 || (p >= 16 && p < 24)) {
        constexpr bool A = p < 8;
        f32x16& Sn = A ? Sb : Sa; f32x16& So = A ? Sa : Sb;
        if constexpr (A || !LAST) { Sn = mfmaL(fr[j], Q[j], j ? Sn : nm); PIN(Sn); }
        if constexpr ((j & 1) == 0) { So[2 * j] = ex2(So[2 * j]); }
        else {
          So[2 * j - 1] = ex2(So[2 * j - 1]); So[2 * j] = ex2(So[2 * j]); So[2 * j + 1] = ex2(So[2 * j + 1]);
          if constexpr (j > 1) { P[j - 3] = cvtpk(So[2 * j - 6], So[2 * j - 5]); P[j - 2] = cvtpk(So[2 * j - 4], So[2 * j - 3]); PIN(P[j - 3]); PIN(P[j - 2]); }
        }
        PIN(So);
      } else {
        constexpr bool A = p < 16;
        f32x16& Sn = A ? Sb : Sa; f32x16& So = A ? Sa : Sb;
        constexpr int t = j >> 2, D = j & 3;
        const u32x4 pw = {P[4 * t], P[4 * t + 1], P[4 * t + 2], P[4 * t + 3]};
        O[D] = mfmaL(fr[j], __builtin_bit_cast(bf16x8, pw), O[D]); PIN(O[D]);
        if constexpr (j == 0) { P[6] = cvtpk(So[12], So[13]); P[7] = cvtpk(So[14], So[15]); PIN(P[6]); PIN(P[7]); }
        if constexpr (j & 1) {
          l0 += So[2 * j - 2]; PIN(l0); l1 += So[2 * j - 1]; PIN(l1); l0 += So[2 * j]; PIN(l0); l1 += So[2 * j + 1]; PIN(l1);
          if constexpr (A || !LAST) {
            if constexpr (j == 1) { mx = imax3(Sn[0], Sn[1], Sn[2]); my = imax3(Sn[3], Sn[4], Sn[5]); }
            if constexpr (j == 3) { mx = imax3(mx, Sn[6], Sn[7]); my = imax3(my, Sn[8], Sn[9]); }
            if constexpr (j == 5) { mx = imax3(mx, Sn[10], Sn[11]); my = imax3(my, Sn[12], Sn[13]); }
            if constexpr (j == 7) { mx = imax3(mx, Sn[14], Sn[15]); mx = max(mx, my); }
            PIN(mx); PIN(my);
          }
        }
        if constexpr (j == 7 && (A || !LAST)) {
          SB();
          if (__builtin_expect(__any(mx > __float_as_int(THR)), 0)) {
            if constexpr (A && !LAST) { PIN(fr[0]); PIN(fr[1]); PIN(fr[2]); PIN(fr[3]); }
            fixL(Sn, false);
          }
        }
        if constexpr (p == 27) {
          const int dv = sv >= NSLOT - 2 ? (2 - NSLOT) * SLOT : 2 * SLOT;
          sv += sv >= NSLOT - 2 ? 2 - NSLOT : 2;
          aV[0] += dv; aV[1] += dv;
          if constexpr (!LAST) { MEMFENCE(); stsw(aMyDone, step - 2); }
        }
      }
      SB();
    });
  };
  for (int n = 0; n < 15; ++n) { body(BoolC<false>{}); }
  body(BoolC<true>{});

  int ln = __builtin_amdgcn_mbcnt_hi(~0u, __builtin_amdgcn_mbcnt_lo(~0u, 0u));
  PIN(ln);
  const unsigned l16 = ln * 16;
  bf16x8 fo[32];
  {
    const float rl = __builtin_amdgcn_rcpf(xadd32(l0 + l1));
    static_for<4>([&](auto dc) {
      constexpr int D = decltype(dc)::value;
      fo[2 * D] = pack8(quad<0>(O[D]) * rl, quad<1>(O[D]) * rl);
      fo[2 * D + 1] = pack8(quad<2>(O[D]) * rl, quad<3>(O[D]) * rl);
      st128(__builtin_bit_cast(u32x4, fo[2 * D]), rof, l16, ((h & 3) * 36 + w * 8 + 2 * D) * 1024, 16);
      st128(__builtin_bit_cast(u32x4, fo[2 * D + 1]), rof, l16, ((h & 3) * 36 + w * 8 + 2 * D + 1) * 1024, 16);
    });
  }
  {
    const unsigned g2 = ln >> 4, q = (ln & 15) >> 2, p = ln & 3, r8a = 4 * (ln >> 5) + q;
    const unsigned v0 = ldsb + r8a * 128 + (((2 * (g2 & 1) + (p >> 1)) ^ mv(r8a)) << 4) + 8 * (p & 1);
    const unsigned v1 = ldsb + r8a * 128 + (((4 + 2 * (g2 & 1) + (p >> 1)) ^ mv(r8a)) << 4) + 8 * (p & 1);
    f32x16 acc[4] = {};
    GAS unsigned* fl2 = flag2 + w * 32 + (h >> 2);
    unsigned av0 = 0, av1 = 0;
    auto rd = [&](auto rc) {
      constexpr int r = decltype(rc)::value;
      if constexpr (r < 128) {
        constexpr int i = r < 32 ? r >> 4 : 2 + ((r - 32) % 24) / 4;
        constexpr int t = r < 32 ? (r >> 2) & 3 : r & 3, D = r < 32 ? r & 3 : (r - 32) / 24;
        const unsigned a = ((D & 1) ? av1 : av0) + t * 4096 + (D >> 1) * 1024;
        fr[r & 7] = cat(ldstr(a), ldstr(a + 2048));
        (void)i;
      }
    };
    wait2(aWOF, 2);
    wait1(aHS, 1);
    static_for<132>([&](auto rc) {
      constexpr int r = decltype(rc)::value, pp = r - 4;
      if constexpr (pp == OWN_AT) {
        wait_vm<0>();
        if (ln == 0) set_flag(flag1 + (h & 3) * 32 + w);
        wait_bits(aRDY, 15u << (4 * w));
#pragma unroll
        for (int k = 8; k < 32; ++k)
          fo[k] = __builtin_bit_cast(bf16x8, __builtin_amdgcn_raw_buffer_load_b128(rof, l16, (((h + (k >> 3)) & 3) * 36 + w * 8 + (k & 7)) * 1024, 16));
      }
      if constexpr (pp >= 0) {
        constexpr int k = pp < 32 ? pp >> 2 : 8 + (pp - 32) % 24, D = pp < 32 ? pp & 3 : (pp - 32) / 24;
        acc[D] = mfmaL(fr[pp & 7], fo[k], acc[D]); PIN(acc[D]);
        SB();
      }
      if constexpr (r == 32) { wait2(aWOF, 8); }
      if constexpr (r < 128) {
        if constexpr ((r < 32 && (r & 15) == 0) || (r >= 32 && (r & 3) == 0)) {
          constexpr int i = r < 32 ? r >> 4 : 2 + ((r - 32) % 24) / 4;
          unsigned so = wo_slot(i);
          asm volatile("" : "+s"(so));
          av0 = v0 + so; av1 = v1 + so;
        }
        rd(IntC<(r ^ 1)>{});
      }
      if constexpr (pp >= 0) {
        constexpr int D = pp < 32 ? pp & 3 : (pp - 32) / 24;
        if constexpr (pp >= 32 && (pp - 32) % 24 == 23) {
          SB();
          if constexpr (D >= 2) {
            wait_vm<(D == 2 ? 2 : 3)>();
            if (ln == 0) set_flag(fl2 + (D - 2) * 8);
          }
          st128(__builtin_bit_cast(u32x4, pack8(quad<0>(acc[D]), quad<1>(acc[D]))), rp, l16, (w * 8 + D * 2) * 1024, 16);
          st128(__builtin_bit_cast(u32x4, pack8(quad<2>(acc[D]), quad<3>(acc[D]))), rp, l16, (w * 8 + D * 2 + 1) * 1024, 16);
          stsw(aMyDone, 41 + D);
        }
      }
      SB();
    });
    wait_vm<3>();
    if (ln == 0) set_flag(fl2 + 2 * 8);
    wait_vm<1>();
    if (ln == 0) set_flag(fl2 + 3 * 8);
  }
}

extern "C" void kernel_launch(void* const* d_in, const int* in_sizes, int n_in, void* d_out, int out_size, void* d_ws,
                              size_t ws_size, hipStream_t stream) {
  const uint16_t* x = (const uint16_t*)d_in[0];
  const uint16_t* kc = (const uint16_t*)d_in[1];
  const uint16_t* vc = (const uint16_t*)d_in[2];
  const uint16_t* wq = (const uint16_t*)d_in[3];
  const uint16_t* wo = (const uint16_t*)d_in[4];
  (void)hipFuncSetAttribute((const void*)attn_kernel, hipFuncAttributeMaxDynamicSharedMemorySize, LDS_BYTES);
  hipLaunchKernelGGL(attn_kernel, dim3(256), dim3(512), LDS_BYTES, stream, x, kc, vc, wq, wo, (char*)d_ws, (uint16_t*)d_out);
}
```
